```python
import torch

def _softmax(x, axis=-1):
    return torch.softmax(x, dim=axis)
def _gelu(x):
    return torch.nn.functional.gelu(x, approximate="tanh")
def _silu(x):
    return torch.nn.functional.silu(x)
def _sigmoid(x):
    return torch.sigmoid(x)
def _layernorm(x, axis=-1, eps=1e-5):
    m = x.mean(dim=axis, keepdim=True)
    v = x.var(dim=axis, keepdim=True, unbiased=False)
    return (x - m) / torch.sqrt(v + eps)
def _scatter_add(tgt, idx, src, axis=0):
    return tgt.clone().index_add_(axis, idx, src)
def _segment_sum(src, seg, n):
    out = torch.zeros((n,) + src.shape[1:], dtype=src.dtype)
    return out.index_add_(0, seg, src)
def _pad(x, pw):
    flat = tuple(v for pair in reversed(pw) for v in pair)
    return torch.nn.functional.pad(x, flat)

kernel_name = 'gated_mix__compressed_kv_cla__dense_activation_squared_relu__decode'

def setup_inputs():

    _w = (lambda a: (lambda t: t.to(torch.bfloat16) if t.is_floating_point() else t)(torch.as_tensor(a)))
    def _A_setup():
        B, H, Sq, Sk, D, M = 2, 32, 1, 8192, 128, 576
        Dc = 4 * D
        return {
            "x": _w((lambda *s: torch.randn(*s, dtype=torch.bfloat16) * 0.05)(B, Sq, M)),
            "c_kv": _w((lambda *s: torch.randn(*s, dtype=torch.bfloat16) * 1.6)(B, Sk, Dc)),
            "w_q": _w((lambda *s: torch.randn(*s, dtype=torch.bfloat16) * 6.4)(M, H, D) * (M ** -0.5)),
            "w_uk": _w((lambda *s: torch.randn(*s, dtype=torch.bfloat16) * 6.4)(Dc, H, D) * (Dc ** -0.5)),
            "w_uv": _w((lambda *s: torch.randn(*s, dtype=torch.bfloat16) * 1.6)(Dc, H, D) * (Dc ** -0.5)),
            "w_o": _w((lambda *s: torch.randn(*s, dtype=torch.bfloat16) * 1.6)(H, D, M) * ((H * D) ** -0.5)),
        }
    _A_in = _A_setup()

    def _B_setup():
        B, S, D, F = 2, 1, 576, 4608
        return {
            "x": _w((lambda *s: torch.randn(*s, dtype=torch.bfloat16) * 0.05)(B, S, D)),
            "w1": _w((lambda *s: torch.randn(*s, dtype=torch.bfloat16) * 0.4)(D, F)),
            "b1": _w((lambda *s: torch.randn(*s, dtype=torch.bfloat16) * 0.2)(F)),
            "w2": _w((lambda *s: torch.randn(*s, dtype=torch.bfloat16) * 0.2)(F, D)),
            "b2": _w((lambda *s: torch.randn(*s, dtype=torch.bfloat16) * 0.2)(D)),
        }
    _B_in = _B_setup()

    return {
        "A_x": _A_in["x"],
        "A_c_kv": _A_in["c_kv"],
        "A_w_q": _A_in["w_q"],
        "A_w_uk": _A_in["w_uk"],
        "A_w_uv": _A_in["w_uv"],
        "A_w_o": _A_in["w_o"],
        "B_x": _B_in["x"],
        "B_w1": _B_in["w1"],
        "B_b1": _B_in["b1"],
        "B_w2": _B_in["w2"],
        "B_b2": _B_in["b2"],
        "gate_w": _w((lambda *s: torch.randn(*s, dtype=torch.bfloat16) * 0.05)(576) * 16.0),
    }

def reference(A_x, A_c_kv, A_w_q, A_w_uk, A_w_uv, A_w_o, B_x, B_w1, B_b1, B_w2, B_b2, gate_w):
    def _A_ref(x, c_kv, w_q, w_uk, w_uv, w_o):
        B, Sq, M = x.shape[0], x.shape[1], x.shape[2]
        Sk = c_kv.shape[1]
        H, D = w_q.shape[1], w_q.shape[2]
        q = torch.einsum("bqm,mhd->bhqd", x, w_q)
        k = torch.einsum("bkc,chd->bhkd", c_kv, w_uk)
        v = torch.einsum("bkc,chd->bhkd", c_kv, w_uv)
        scores = torch.einsum("bhqd,bhkd->bhqk", q, k) * (D ** -0.5)
        off = Sk - Sq
        qi = torch.arange(Sq)[:, None]
        kj = torch.arange(Sk)[None, :]
        cmask = kj <= (qi + off)
        scores = torch.where(cmask[None, None], scores, -1e9)
        p = _softmax(scores, axis=-1)
        ctx = torch.einsum("bhqk,bhkd->bhqd", p, v)
        return torch.einsum("bhqd,hdm->bqm", ctx, w_o)

    def _B_ref(x, w1, b1, w2, b2):
        h = torch.einsum("bsd,df->bsf", x, w1) + b1
        zero = torch.zeros_like(h)
        r = torch.where(h > zero, h, zero)
        h = r * r
        out = torch.einsum("bsf,fd->bsd", h, w2) + b2
        return out

    x = A_x
    g = _sigmoid((x * gate_w).sum(-1, keepdims=True))
    a = _A_ref(x, A_c_kv, A_w_q, A_w_uk, A_w_uv, A_w_o)
    b = _B_ref(x, B_w1, B_b1, B_w2, B_b2)
    return g * a + (1 - g) * b
```

```cpp
#include <hip/hip_runtime.h>
#include <hip/hip_bf16.h>
#include <stdint.h>

typedef unsigned short u16;
__device__ __forceinline__ float bf2f(u16 v) { return __uint_as_float(((unsigned)v) << 16); }
__device__ __forceinline__ u16 f2bf(float f) { unsigned u = __float_as_uint(f); u += 0x7fff + ((u >> 16) & 1); return (u16)(u >> 16); }

constexpr int B = 2, H = 32, SK = 8192, D = 128, M = 576, DC = 512, F = 4608;
constexpr int NSPLIT = 128, KPS = SK / NSPLIT;

__global__ void __launch_bounds__(256) k1(const u16* x, const u16* wq, const u16* w1, const u16* b1, float* q, float* h1) {
  int col = blockIdx.x * 64 + (threadIdx.x & 63);
  int kg = threadIdx.x >> 6;
  __shared__ float red[4][64][2];
  float a0 = 0, a1 = 0;
  if (col < 4096) { for (int m = kg; m < M; m += 4) { float w = bf2f(wq[(size_t)m * 4096 + col]); a0 += bf2f(x[m]) * w; a1 += bf2f(x[M + m]) * w; } }
  else { int f = col - 4096; for (int m = kg; m < M; m += 4) { float w = bf2f(w1[(size_t)m * F + f]); a0 += bf2f(x[m]) * w; a1 += bf2f(x[M + m]) * w; } }
  red[kg][threadIdx.x & 63][0] = a0; red[kg][threadIdx.x & 63][1] = a1;
  __syncthreads();
  if (kg == 0) {
    int l = threadIdx.x;
    a0 = red[0][l][0] + red[1][l][0] + red[2][l][0] + red[3][l][0];
    a1 = red[0][l][1] + red[1][l][1] + red[2][l][1] + red[3][l][1];
    if (col < 4096) { q[col] = a0; q[4096 + col] = a1; }
    else { int f = col - 4096; float bb = bf2f(b1[f]); a0 += bb; a1 += bb; a0 = fmaxf(a0, 0.f); a1 = fmaxf(a1, 0.f); h1[f] = a0 * a0; h1[F + f] = a1 * a1; }
  }
}
__global__ void __launch_bounds__(256) k2(const float* q, const u16* wuk, float* qabs) {
  int row = blockIdx.x * 4 + (threadIdx.x >> 6); int lane = threadIdx.x & 63;
  int c = row / H, h = row % H;
  float w0 = bf2f(wuk[(size_t)row * D + lane * 2]), w1 = bf2f(wuk[(size_t)row * D + lane * 2 + 1]);
  float a0 = q[h * D + lane * 2] * w0 + q[h * D + lane * 2 + 1] * w1;
  float a1 = q[4096 + h * D + lane * 2] * w0 + q[4096 + h * D + lane * 2 + 1] * w1;
  for (int o = 32; o; o >>= 1) { a0 += __shfl_xor(a0, o); a1 += __shfl_xor(a1, o); }
  if (lane == 0) { const float sc = 0.08838834764831845f; qabs[(0 * H + h) * DC + c] = a0 * sc; qabs[(1 * H + h) * DC + c] = a1 * sc; }
}
__global__ void __launch_bounds__(256) k3(const float* qabs, const u16* ckv, float* po, float* pml) {
  int s = blockIdx.x, b = blockIdx.y, t = threadIdx.x;
  __shared__ float S[H][KPS];
  __shared__ float mm[H], ll[H];
  const u16* kv = ckv + ((size_t)b * SK + (size_t)s * KPS) * DC;
  for (int i = t; i < H * KPS; i += 256) {
    int h = i / KPS, k = i % KPS; float a = 0;
    for (int c = 0; c < DC; ++c) a += qabs[(b * H + h) * DC + c] * bf2f(kv[k * DC + c]);
    S[h][k] = a;
  }
  __syncthreads();
  if (t < H) { float m = -INFINITY; for (int k = 0; k < KPS; ++k) m = fmaxf(m, S[t][k]); float l = 0; for (int k = 0; k < KPS; ++k) { float p = __expf(S[t][k] - m); S[t][k] = p; l += p; } mm[t] = m; ll[t] = l; }
  __syncthreads();
  for (int i = t; i < H * DC; i += 256) {
    int h = i / DC, c = i % DC; float a = 0;
    for (int k = 0; k < KPS; ++k) a += S[h][k] * bf2f(kv[k * DC + c]);
    po[(((size_t)b * H + h) * NSPLIT + s) * DC + c] = a;
  }
  if (t < H) { pml[(((size_t)b * H + t) * NSPLIT + s) * 2] = mm[t]; pml[(((size_t)b * H + t) * NSPLIT + s) * 2 + 1] = ll[t]; }
}
__global__ void __launch_bounds__(512) k4(const float* po, const float* pml, const u16* wuv, float* ctx) {
  int h = blockIdx.x, b = blockIdx.y, t = threadIdx.x;
  __shared__ float cc[DC]; __shared__ float red[4][D];
  const float* ml = pml + ((size_t)b * H + h) * NSPLIT * 2;
  float M_ = -INFINITY; for (int s = 0; s < NSPLIT; ++s) M_ = fmaxf(M_, ml[2 * s]);
  float L = 0, a = 0;
  for (int s = 0; s < NSPLIT; ++s) { float w = __expf(ml[2 * s] - M_); L += w * ml[2 * s + 1]; a += w * po[(((size_t)b * H + h) * NSPLIT + s) * DC + t]; }
  cc[t] = a / L;
  __syncthreads();
  int d = t & 127, g = t >> 7; float o = 0;
  for (int c = g * 128; c < g * 128 + 128; ++c) o += cc[c] * bf2f(wuv[((size_t)c * H + h) * D + d]);
  red[g][d] = o; __syncthreads();
  if (t < D) ctx[(b * H + h) * D + t] = red[0][t] + red[1][t] + red[2][t] + red[3][t];
}
__global__ void __launch_bounds__(576) k5(const float* ctx, const float* h1, const u16* wo, const u16* w2, float* part) {
  int blk = blockIdx.x, m = threadIdx.x; float a0 = 0, a1 = 0;
  for (int r = blk * 64; r < blk * 64 + 64; ++r) {
    if (r < 4096) { float w = bf2f(wo[(size_t)r * M + m]); a0 += ctx[r] * w; a1 += ctx[4096 + r] * w; }
    else { int f = r - 4096; float w = bf2f(w2[(size_t)f * M + m]); a0 += h1[f] * w; a1 += h1[F + f] * w; }
  }
  part[(blk * 2 + 0) * M + m] = a0; part[(blk * 2 + 1) * M + m] = a1;
}
__global__ void __launch_bounds__(576) k6(const float* part, const u16* x, const u16* gw, const u16* b2, u16* out) {
  int b = blockIdx.x, m = threadIdx.x;
  __shared__ float red[576];
  red[m] = bf2f(x[b * M + m]) * bf2f(gw[m]); __syncthreads();
  float g = 0; for (int i = 0; i < M; ++i) g += red[i];
  g = 1.f / (1.f + __expf(-g));
  float a = 0, f = 0;
  for (int i = 0; i < 64; ++i) a += part[(i * 2 + b) * M + m];
  for (int i = 64; i < 136; ++i) f += part[(i * 2 + b) * M + m];
  f += bf2f(b2[m]);
  out[b * M + m] = f2bf(g * a + (1.f - g) * f);
}

extern "C" void kernel_launch(void* const* d_in, const int* in_sizes, int n_in, void* d_out, int out_size, void* d_ws, size_t ws_size, hipStream_t stream) {
  const u16* x = (const u16*)d_in[0]; const u16* ckv = (const u16*)d_in[1]; const u16* wq = (const u16*)d_in[2]; const u16* wuk = (const u16*)d_in[3];
  const u16* wuv = (const u16*)d_in[4]; const u16* wo = (const u16*)d_in[5]; const u16* w1 = (const u16*)d_in[7]; const u16* b1 = (const u16*)d_in[8];
  const u16* w2 = (const u16*)d_in[9]; const u16* b2 = (const u16*)d_in[10]; const u16* gw = (const u16*)d_in[11];
  float* ws = (float*)d_ws;
  float* q = ws; float* h1 = q + 8192; float* qabs = h1 + 2 * F; float* ctx = qabs + 2 * H * DC; float* part = ctx + 8192; float* pml = part + 136 * 2 * M; float* po = pml + 2 * H * NSPLIT * 2 + 64;
  k1<<<136, 256, 0, stream>>>(x, wq, w1, b1, q, h1);
  k2<<<4096, 256, 0, stream>>>(q, wuk, qabs);
  k3<<<dim3(NSPLIT, 2), 256, 0, stream>>>(qabs, ckv, po, pml);
  k4<<<dim3(H, 2), 512, 0, stream>>>(po, pml, wuv, ctx);
  k5<<<136, 576, 0, stream>>>(ctx, h1, wo, w2, part);
  k6<<<2, 576, 0, stream>>>(part, x, gw, b2, (u16*)d_out);
}
```

```cpp
#include <hip/hip_runtime.h>
#include <hip/hip_bf16.h>
#include <stdint.h>

typedef unsigned short u16;
typedef unsigned long long u64;
typedef unsigned v4u __attribute__((ext_vector_type(4)));
typedef unsigned v2u __attribute__((ext_vector_type(2)));
typedef float f32x4 __attribute__((ext_vector_type(4)));
typedef float f32x2 __attribute__((ext_vector_type(2)));
typedef short bf16x8 __attribute__((ext_vector_type(8)));
typedef short s16x4 __attribute__((ext_vector_type(4)));
#define LAS __attribute__((address_space(3)))
#define GAS __attribute__((address_space(1)))
#define DI __device__ __forceinline__

namespace {
constexpr int MD = 576, NF = 4608;
constexpr int WS_EPOCH = 0;
constexpr int WS_QA = 4096;
constexpr int WS_HX = WS_QA + 512 * 384;
constexpr int WS_PM = WS_HX + 216 * 1152;
constexpr int WS_PO = WS_PM + 128 * 8192;
constexpr int WS_FO = WS_PO + 128 * 32768;
constexpr int WS_END = WS_FO + 96 * 16384;
constexpr int WS_STAMP = WS_END + 4096;
constexpr int L_KV = 0;
constexpr int L_QA = 131072;
constexpr int L_CP = L_QA, L_XP = L_QA + 8192;
constexpr int L_P = L_QA + 16384;
constexpr int L_QP = L_P;
constexpr int L_OT = L_KV;
constexpr int L_OF = L_P + 4096;
constexpr int L_X = L_OF + 4608;
constexpr int L_QB = L_X + 3072;
constexpr int L_HID = L_QB + 512;
constexpr int L_S0 = L_HID + 256;
constexpr int L_SM = L_S0;
constexpr int L_SS = L_SM + 512;
constexpr int L_CS = L_SS + 512;
constexpr int L_CTX = L_CS + 128;
constexpr int L_G = L_CTX + 512;
constexpr int L_FL = L_G + 16;
constexpr int F_QP = L_FL, F_QA = L_FL + 32, F_KV = L_FL + 64, F_XP = L_FL + 96, F_OUT = L_FL + 128, F_FIN = L_FL + 160, F_MISC = L_FL + 192;
constexpr int F_X = F_MISC, F_ISS = F_MISC + 4, F_SENT = F_MISC + 8, F_DMAI = F_MISC + 12, F_HID = F_MISC + 16, F_GO = F_MISC + 20, F_PUB = F_MISC + 24, F_H2 = F_MISC + 28;
constexpr int L_S1 = L_FL + 224;
constexpr int L_FIN = L_S1;
constexpr int L_END = L_FIN + 512;
static_assert(L_END <= 163840 && (L_S1 - L_S0) % 16 == 0 && (L_S1 - L_S0) / 16 <= 128, "lds");
#define SPINCAP (1u << 18)
#define GOEARLY 1
#define SPN 1
#define SLT 6
#define ST1 6
#define ST2 11
#define SLH1 4
#define SLH2 24
#define SLH3 16
constexpr unsigned SPIN_CAP = SPINCAP, SENT = 0xFFFFFFFFu;
constexpr int AUX_SC1 = 16;

struct Args {
  const u16 *x, *ckv, *wq, *wuk, *wuv, *wo, *w1, *b1, *w2, *b2, *gw;
  u16* out;
  unsigned char* ws;
};

DI void bar() { asm volatile("s_waitcnt lgkmcnt(0)" ::: "memory"); __builtin_amdgcn_s_barrier(); asm volatile("" ::: "memory"); }
DI void lds_wait(LAS unsigned char* L, int off, unsigned want) {
  while (__hip_atomic_load((LAS unsigned*)(L + off), __ATOMIC_RELAXED, __HIP_MEMORY_SCOPE_WORKGROUP) < want) __builtin_amdgcn_s_sleep(1);
  asm volatile("" ::: "memory");
}
DI void lds_wait_fast(LAS unsigned char* L, int off, unsigned want) {
  while (__hip_atomic_load((LAS unsigned*)(L + off), __ATOMIC_RELAXED, __HIP_MEMORY_SCOPE_WORKGROUP) < want) { }
  asm volatile("" ::: "memory");
}
DI void lds_wait_slow(LAS unsigned char* L, int off, unsigned want) {
  while (__hip_atomic_load((LAS unsigned*)(L + off), __ATOMIC_RELAXED, __HIP_MEMORY_SCOPE_WORKGROUP) < want) __builtin_amdgcn_s_sleep(4);
  asm volatile("" ::: "memory");
}
DI void lds_set(LAS unsigned char* L, int off, int lane) {
  asm volatile("s_waitcnt lgkmcnt(0)" ::: "memory");
  if (lane == 0) (void)__hip_atomic_fetch_add((LAS unsigned*)(L + off), 1u, __ATOMIC_RELAXED, __HIP_MEMORY_SCOPE_WORKGROUP);
}
DI v4u ldnt(const void* p) { return *(const GAS v4u*)p; }
DI f32x4 mfma(v4u a, v4u b, f32x4 c) { return __builtin_amdgcn_mfma_f32_16x16x32_bf16(__builtin_bit_cast(bf16x8, a), __builtin_bit_cast(bf16x8, b), c, 0, 0, 0); }
DI float bflo(unsigned u) { return __uint_as_float(u << 16); }
DI float bfhi(unsigned u) { return __uint_as_float(u & 0xffff0000u); }
DI float bf1(u16 v) { return __uint_as_float(((unsigned)v) << 16); }
DI unsigned pk2(float lo, float hi) {
  typedef __bf16 bf2 __attribute__((ext_vector_type(2)));
  bf2 r; r.x = (__bf16)lo; r.y = (__bf16)hi; return __builtin_bit_cast(unsigned, r);
}
DI unsigned rotl(unsigned x, unsigned r) { return __builtin_amdgcn_alignbit(x, x, 32u - r); }
template <int CTRL> DI unsigned dppu(unsigned x) { return (unsigned)__builtin_amdgcn_mov_dpp((int)x, CTRL, 0xf, 0xf, true); }
template <int CTRL> DI float dppf(float x) { return __uint_as_float(dppu<CTRL>(__float_as_uint(x))); }
constexpr int XOR1 = 0xB1, XOR2 = 0x4E, HMIR = 0x141, ROR4 = 0x124, ROR8 = 0x128;
DI unsigned x16addu(unsigned x) { auto s = __builtin_amdgcn_permlane16_swap(x, x, false, false); return s[0] + s[1]; }
DI unsigned x32addu(unsigned x) { auto s = __builtin_amdgcn_permlane32_swap(x, x, false, false); return s[0] + s[1]; }
DI float x16addf(float x) { auto s = __builtin_amdgcn_permlane16_swap(__float_as_uint(x), __float_as_uint(x), false, false); return __uint_as_float(s[0]) + __uint_as_float(s[1]); }
DI float x32addf(float x) { auto s = __builtin_amdgcn_permlane32_swap(__float_as_uint(x), __float_as_uint(x), false, false); return __uint_as_float(s[0]) + __uint_as_float(s[1]); }
DI float x16maxf(float x) { auto s = __builtin_amdgcn_permlane16_swap(__float_as_uint(x), __float_as_uint(x), false, false); return fmaxf(__uint_as_float(s[0]), __uint_as_float(s[1])); }
DI float x32maxf(float x) { auto s = __builtin_amdgcn_permlane32_swap(__float_as_uint(x), __float_as_uint(x), false, false); return fmaxf(__uint_as_float(s[0]), __uint_as_float(s[1])); }
DI unsigned wsum(unsigned x) { x += dppu<XOR1>(x); x += dppu<XOR2>(x); x += dppu<ROR4>(x); x += dppu<ROR8>(x); return x32addu(x16addu(x)); }
DI float wsumf(float x) { x += dppf<XOR1>(x); x += dppf<XOR2>(x); x += dppf<ROR4>(x); x += dppf<ROR8>(x); return x32addf(x16addf(x)); }
DI unsigned d2(unsigned a, unsigned k, unsigned c) { typedef unsigned short us2 __attribute__((ext_vector_type(2)));
  return __builtin_amdgcn_udot2(__builtin_bit_cast(us2, a), __builtin_bit_cast(us2, k), c, false); }
DI void cs(v4u a, unsigned& U, unsigned& V) {
  U = d2(a.x, 0x9E37B5C5u, U); U = d2(a.y, 0x7A4DC2B3u, U); U = d2(a.z, 0xE4F1594Du, U); U = d2(a.w, 0x3B8FD16Fu, U);
  V = d2(a.x, 0x62E9F2A7u, V); V = d2(a.y, 0xC96B4F1Du, V); V = d2(a.z, 0x5573A9E5u, V); V = d2(a.w, 0xED2F8799u, V);
}
DI v4u lds_v4(LAS unsigned char* L, int off) { return *(volatile LAS v4u*)(L + off); }
DI bool nos(v4u v) { return (v.x != SENT) & (v.y != SENT) & (v.z != SENT) & (v.w != SENT); }
template <int N, int SLEEP> DI void flag_wait(LAS unsigned char* L, int off) {
  for (;;) { bool ok = nos(lds_v4(L, off)); if (N > 4) { const v4u b = lds_v4(L, off + 16); ok &= (b.x != SENT) & (N < 6 || b.y != SENT) & (N < 7 || b.z != SENT) & (N < 8 || b.w != SENT); }
    if (ok) break; if (SLEEP) __builtin_amdgcn_s_sleep(SLEEP); }
  asm volatile("" ::: "memory");
}
DI void flag_set(LAS unsigned char* L, int off, int lane) { asm volatile("" ::: "memory"); if (lane == 0) *(volatile LAS unsigned*)(L + off) = 1u; asm volatile("" ::: "memory"); }
template <int SLEEP> DI void cnt_wait(LAS unsigned char* L, int off, unsigned n) {
  while (*(volatile LAS unsigned*)(L + off) + 1u < n) { if (SLEEP) __builtin_amdgcn_s_sleep(SLEEP); }
  asm volatile("" ::: "memory");
}
DI void cnt_add(LAS unsigned char* L, int off, int lane) { asm volatile("" ::: "memory"); if (lane == 0) (void)__hip_atomic_fetch_add((LAS unsigned*)(L + off), 1u, __ATOMIC_RELAXED, __HIP_MEMORY_SCOPE_WORKGROUP); asm volatile("" ::: "memory"); }
struct Diag { unsigned m0, m1, m2, m3, sh; };
DI Diag make_diag(int i) { const int p = i & 7; Diag d; d.sh = (p & 1) * 16; d.m0 = (p >> 1) == 0 ? ~0u : 0u; d.m1 = (p >> 1) == 1 ? ~0u : 0u; d.m2 = (p >> 1) == 2 ? ~0u : 0u; d.m3 = (p >> 1) == 3 ? ~0u : 0u; return d; }
DI v4u diag(const Diag& d, unsigned vb16) { const unsigned v = vb16 << d.sh; return v4u{v & d.m0, v & d.m1, v & d.m2, v & d.m3}; }
DI unsigned bfr(float f) { return pk2(f, 0.f) & 0xffffu; }
DI void glds16_nt(const void* gsrc, unsigned lds_dst) {
  unsigned keep;
  asm volatile("s_mov_b32 %0, m0\n\ts_mov_b32 m0, %2\n\ts_nop 0\n\tglobal_load_lds_dwordx4 %1, off\n\ts_mov_b32 m0, %0" : "=&s"(keep) : "v"(gsrc), "s"(lds_dst) : "memory");
}
#define LD128(off) __builtin_amdgcn_raw_buffer_load_b128(rs, (off), 0, AUX_SC1)
#define LD64(off) __builtin_amdgcn_raw_buffer_load_b64(rs, (off), 0, AUX_SC1)
#define ST128(v, off) __builtin_amdgcn_raw_buffer_store_b128((v), rs, (off), 0, AUX_SC1)
#define ST64(v, off) __builtin_amdgcn_raw_buffer_store_b64((v), rs, (off), 0, AUX_SC1)
#define VMWAIT(N) do { __builtin_amdgcn_sched_barrier(0); asm volatile("s_waitcnt vmcnt(%0)" :: "n"(N) : "memory"); __builtin_amdgcn_sched_barrier(0); } while (0)
#define STAMP(k) do { } while (0)
#define STAMP_OUT() do { } while (0)

__global__ void __launch_bounds__(1024) fused_kernel(Args a) {
  extern __shared__ __attribute__((aligned(16))) unsigned char smem[];
  LAS unsigned char* L = (LAS unsigned char*)smem;
  const int tid = threadIdx.x, lane = tid & 63, wave = __builtin_amdgcn_readfirstlane(tid >> 6);
  const int wg = blockIdx.x, xl = wg & 7, r = wg >> 3;
  const int n = lane & 15, g = lane >> 4;
  const int h = xl + 8 * (r >> 3), j = r & 7, cq = j >> 1, dh = j & 1;
  const int hgA = r >> 4, kb = xl * 16 + (r & 15), cb = kb >> 6;
  const __amdgpu_buffer_rsrc_t rs = __builtin_amdgcn_make_buffer_rsrc(a.ws, 0, WS_END, 0x00020000);
  const Diag dg = make_diag(n);
  const f32x4 z4 = {0.f, 0.f, 0.f, 0.f};
  STAMP(0);
  if (wave == 15) {
    const unsigned char* xs = (const unsigned char*)a.x;
    glds16_nt(xs + lane * 16, (unsigned)(size_t)(L + L_X)); glds16_nt(xs + 1024 + lane * 16, (unsigned)(size_t)(L + L_X) + 1024);
    glds16_nt(xs + 2048 + (lane < 16 ? lane : 15) * 16, (unsigned)(size_t)(L + L_X) + 2048); }
  if (tid < (L_S1 - L_S0) / 16) *(LAS v4u*)(L + L_S0 + tid * 16) = v4u{SENT, SENT, SENT, SENT};
  bar();

  if (wave < 8) {
    const int w = wave;
    __builtin_amdgcn_s_setprio(3);
    { v4u wv[9];
      const u16* p = a.wq + (size_t)(2 * g + (n >> 3)) * 4096 + h * 128 + dh * 64 + 8 * (n & 7);
#pragma unroll
      for (int k = 0; k < 9; ++k) { int I = w + 8 * k + 18 * cq; I = I >= 72 ? I - 72 : I; wv[k] = ldnt(p + (size_t)I * (8 * 4096)); }
      __builtin_amdgcn_sched_barrier(0);
      cnt_add(L, F_ISS, lane);
      STAMP(8);
      cnt_wait<1>(L, F_X, 1u);
      unsigned xv[9];
#pragma unroll
      for (int k = 0; k < 9; ++k) { int I = w + 8 * k + 18 * cq; I = I >= 72 ? I - 72 : I; xv[k] = *(const LAS unsigned*)(L + L_X + (n >> 3) * 1152 + (8 * I + 2 * g) * 2); }
      f32x4 a0 = z4, a1 = z4;
#pragma unroll
      for (int k = 0; k < 9; ++k) { a0 = mfma(diag(dg, xv[k] & 0xffffu), wv[k], a0); a1 = mfma(diag(dg, xv[k] >> 16), wv[k], a1); }
      f32x4 v = n < 8 ? a0 : a1;
#pragma unroll
      for (int k = 0; k < 4; ++k) v[k] += dppf<ROR8>(v[k]);
      if (n < 8) *(LAS f32x4*)(L + L_QP + ((w * 2 + (g >> 1)) * 64 + 8 * n + 4 * (g & 1)) * 4) = v;
      flag_set(L, F_QP + 4 * w, lane); }
    STAMP(1);
    const u64 ep = __hip_atomic_load((const GAS u64*)(a.ws + WS_EPOCH), __ATOMIC_RELAXED, __HIP_MEMORY_SCOPE_AGENT) + 1ull;
    const unsigned elo = (unsigned)ep, ehi = (unsigned)(ep >> 32);
    __builtin_amdgcn_s_setprio(0);
    cnt_wait<2>(L, F_SENT, 2u);
    flag_wait<4, 2>(L, F_KV);
    __builtin_amdgcn_s_setprio(3);
    v4u kf[16];
    { const int k8 = lane & 7, hq = lane >> 3;
      const int qb = WS_QA + ((cb * 32 + 16 * hgA + hq) * 8 + w) * 384 + k8 * 16, mo = WS_QA + ((cb * 32 + 16 * hgA + n) * 8 + w) * 384 + 256;
      v4u v0, v1, v2, v3; unsigned spins = 0;
      for (;;) {
        asm volatile("" ::: "memory");
        v0 = LD128(qb); v1 = LD128(qb + 128); v2 = LD128(qb + 8 * 3072); v3 = LD128(qb + 8 * 3072 + 128);
        const v4u mt = LD128(mo);
        __builtin_amdgcn_sched_barrier(0);
        if (spins == 0) {
          cnt_add(L, F_GO, lane);
          const int f = ((n & 3) << 2) | (n >> 2);
          const int rowb = L_KV + (16 * w + n) * 1024;
#pragma unroll
          for (int s = 0; s < 16; ++s) kf[s] = *(const LAS v4u*)(L + rowb + (s >> 2) * 256 + (((4 * g + (s & 3)) ^ f) * 16)); }
        __builtin_amdgcn_sched_barrier(0);
        unsigned U = 0, V = 0; cs(v0, U, V); cs(v1, U, V); cs(v2, U, V); cs(v3, U, V);
        if (lane < 16) { U -= mt.x + mt.z - 2u * elo; V -= mt.y + mt.w - 2u * ehi; }
        if ((wsum(U) | wsum(V)) == 0u || ++spins > SPIN_CAP) break;
        __builtin_amdgcn_s_sleep(6);
      }
      auto add2 = [](v4u p, v4u q) { v4u o;
        o.x = pk2(bflo(p.x) + bflo(q.x), bfhi(p.x) + bfhi(q.x)); o.y = pk2(bflo(p.y) + bflo(q.y), bfhi(p.y) + bfhi(q.y));
        o.z = pk2(bflo(p.z) + bflo(q.z), bfhi(p.z) + bfhi(q.z)); o.w = pk2(bflo(p.w) + bflo(q.w), bfhi(p.w) + bfhi(q.w)); return o; };
      const int wb = L_QA + (4 * (w >> 1) + (k8 & 3)) * 1024 + (2 * (w & 1) + (k8 >> 2)) * 256, rot = hq + 4 * (k8 & 3) + 2 * (w & 1);
      *(LAS v4u*)(L + wb + (rot & 15) * 16) = add2(v0, v1);
      *(LAS v4u*)(L + wb + ((rot + 8) & 15) * 16) = add2(v2, v3);
      flag_set(L, F_QA + 4 * w, lane);
    }
    STAMP(2);
    flag_wait<8, SPN>(L, F_QA);
    STAMP(3);
    f32x4 sc = z4, sc2 = z4;
#pragma unroll
    for (int hs = 0; hs < 2; ++hs) {
      v4u qf[8];
#pragma unroll
      for (int s = 0; s < 8; ++s) qf[s] = *(const LAS v4u*)(L + L_QA + (8 * hs + s) * 1024 + g * 256 + ((n + 4 * (s & 3) + 2 * (g >> 1)) & 15) * 16);
      __builtin_amdgcn_sched_barrier(0);
#pragma unroll
      for (int s = 0; s < 8; s += 2) { sc = mfma(kf[8 * hs + s], qf[s], sc); sc2 = mfma(kf[8 * hs + s + 1], qf[s + 1], sc2); }
    }
    sc += sc2;
    { float m = fmaxf(fmaxf(sc.x, sc.y), fmaxf(sc.z, sc.w)); m = x32maxf(x16maxf(m));
      if (g == 0) *(volatile LAS unsigned*)(L + L_SM + n * 32 + w * 4) = min(__float_as_uint(m), SENT - 1u); }
    float mcu;
    { v4u m0, m1;
      for (;;) { m0 = lds_v4(L, L_SM + n * 32); m1 = lds_v4(L, L_SM + n * 32 + 16); if (__all(nos(m0) & nos(m1))) break; __builtin_amdgcn_s_sleep(SPN); }
      STAMP(9);
      const f32x4 f0 = __builtin_bit_cast(f32x4, m0), f1 = __builtin_bit_cast(f32x4, m1);
      mcu = fmaxf(fmaxf(fmaxf(f0.x, f0.y), fmaxf(f0.z, f0.w)), fmaxf(fmaxf(f1.x, f1.y), fmaxf(f1.z, f1.w)));
      const unsigned p01 = pk2(__builtin_amdgcn_exp2f(sc.x - mcu), __builtin_amdgcn_exp2f(sc.y - mcu));
      const unsigned p23 = pk2(__builtin_amdgcn_exp2f(sc.z - mcu), __builtin_amdgcn_exp2f(sc.w - mcu));
      *(volatile LAS v2u*)(L + L_P + (w >> 1) * 1024 + (n + 16 * (2 * (w & 1) + (g >> 1))) * 16 + 8 * (g & 1)) = v2u{p01, p23};
      float l = (bflo(p01) + bfhi(p01)) + (bflo(p23) + bfhi(p23)); l = x32addf(x16addf(l));
      if (g == 0) *(volatile LAS unsigned*)(L + L_SS + n * 32 + w * 4) = min(__float_as_uint(l), SENT - 1u); }
    { const int q4 = n >> 2, p4 = n & 3;
      const unsigned base = L_KV + (8 * g + q4) * 1024 + (w >> 1) * 256 + ((((8 * (w & 1)) + (p4 >> 1)) ^ ((q4 << 2) | (2 * (g & 1)))) * 16) + 8 * (p4 & 1);
      bf16x8 vf[16];
#pragma unroll
      for (int ks = 0; ks < 4; ++ks)
#pragma unroll
        for (int T = 0; T < 4; ++T) {
          const s16x4 lo = __builtin_amdgcn_ds_read_tr16_b64_v4i16((LAS s16x4*)(L + ((base ^ (unsigned)(T << 5)) + ks * 32768)));
          const s16x4 hi = __builtin_amdgcn_ds_read_tr16_b64_v4i16((LAS s16x4*)(L + ((base ^ (unsigned)((T << 5) | (1 << 4) | (1 << 12))) + ks * 32768)));
          vf[ks * 4 + T] = __builtin_shufflevector(lo, hi, 0, 1, 2, 3, 4, 5, 6, 7); }
      __builtin_amdgcn_sched_barrier(0);
      v4u l0, l1;
      for (;;) { l0 = lds_v4(L, L_SS + n * 32); l1 = lds_v4(L, L_SS + n * 32 + 16); if (__all(nos(l0) & nos(l1))) break; __builtin_amdgcn_s_sleep(SPN); }
      asm volatile("" ::: "memory");
      STAMP(10);
      f32x4 o[4] = {z4, z4, z4, z4};
      v4u pf[4];
#pragma unroll
      for (int ks = 0; ks < 4; ++ks) pf[ks] = *(const LAS v4u*)(L + L_P + ks * 1024 + lane * 16);
#pragma unroll
      for (int ks = 0; ks < 4; ++ks)
#pragma unroll
        for (int T = 0; T < 4; ++T) o[T] = __builtin_amdgcn_mfma_f32_16x16x32_bf16(vf[ks * 4 + T], __builtin_bit_cast(bf16x8, pf[ks]), o[T], 0, 0, 0);
      const f32x4 f0 = __builtin_bit_cast(f32x4, l0), f1 = __builtin_bit_cast(f32x4, l1);
      const float lcu = ((f0.x + f0.y) + (f0.z + f0.w)) + ((f1.x + f1.y) + (f1.z + f1.w));
      const int row = ((16 * hgA + n) * 4 + (w >> 1)) * 128 + kb;
      asm volatile("" :: "v"(o[3]));
      STAMP(13);
      __builtin_amdgcn_sched_barrier(0);
      unsigned U = 0, V = 0;
#pragma unroll
      for (int T = 0; T < 4; T += 2) {
        unsigned a0 = pk2(o[T].x, o[T].y), a1 = pk2(o[T].z, o[T].w), b0 = pk2(o[T + 1].x, o[T + 1].y), b1 = pk2(o[T + 1].z, o[T + 1].w);
        auto s0 = __builtin_amdgcn_permlane16_swap(a0, b0, false, false); auto s1 = __builtin_amdgcn_permlane16_swap(a1, b1, false, false);
        const v4u ov = {s0[0], s1[0], s0[1], s1[1]};
        ST128(ov, WS_PO + row * 256 + (8 * (w & 1) + 2 * (T + (g & 1)) + (g >> 1)) * 16);
        cs(ov, U, V);
      }
      __builtin_amdgcn_sched_barrier(0);
      STAMP(14);
      U = x32addu(x16addu(U)); V = x32addu(x16addu(V));
      const unsigned mb = __float_as_uint(mcu), lb = __float_as_uint(lcu);
      if (g < 2) ST128((g == 0 ? v4u{mb, lb, d2(mb, 0x2B5D71C3u, d2(lb, 0x8F63E917u, U + elo)), d2(mb, 0xD1A34E8Bu, d2(lb, 0x47F92C6Du, V + ehi))} : v4u{elo, ehi, elo, ehi}), WS_PM + (row * 2 + (w & 1)) * 32 + g * 16); }
    cnt_add(L, F_PUB, lane);
    STAMP(4);
    v4u uv[2];
#pragma unroll
    for (int t = 0; t < 2; ++t) uv[t] = ldnt(a.wuv + ((size_t)(128 * cq + 16 * w + 8 * t + 2 * g + (n >> 3)) * 32 + h) * 128 + 64 * dh + 8 * (n & 7));
    __builtin_amdgcn_sched_barrier(0);
    { const int row0 = (h * 4 + cq) * 128 + 8 * w + g;
      v4u p[4], mt[4]; unsigned spins = 0;
      __builtin_amdgcn_s_sleep(SLH2);
      for (;;) {
        asm volatile("" ::: "memory");
#pragma unroll
        for (int t = 0; t < 4; ++t) { const int row = row0 + 4 * (t & 1) + 64 * (t >> 1); p[t] = LD128(WS_PO + row * 256 + n * 16); mt[t] = LD128(WS_PM + (row * 2 + (n >> 3)) * 32); }
        __builtin_amdgcn_sched_barrier(0);
        if (spins == 0) cnt_add(L, F_H2, lane);
        __builtin_amdgcn_sched_barrier(0);
        unsigned U = 0, V = 0, Um = 0, Vm = 0;
#pragma unroll
        for (int t = 0; t < 4; ++t) { cs(p[t], U, V);
          Um = d2(mt[t].x, 0x2B5D71C3u, d2(mt[t].y, 0x8F63E917u, Um - mt[t].z)); Vm = d2(mt[t].x, 0xD1A34E8Bu, d2(mt[t].y, 0x47F92C6Du, Vm - mt[t].w)); }
        if ((n & 7) == 0) { U += Um + 4u * elo; V += Vm + 4u * ehi; }
        if ((wsum(U) | wsum(V)) == 0u || ++spins > SPIN_CAP) break;
        const int to = WS_PM + ((((h * 4 + cq) * 128 + 64 * (lane >> 5) + 8 * w) * 2) * 32) + (lane & 31) * 16;
        for (;;) {
          __builtin_amdgcn_s_sleep(SLT);
          asm volatile("" ::: "memory");
          const v2u tg = LD64(to);
          if (__all((lane & 1) == 0 || (tg.x == elo && tg.y == ehi)) || ++spins > SPIN_CAP) break;
        }
      }
      STAMP(5);
      f32x4 acc = z4; float Mb[2], Lb[2];
#pragma unroll
      for (int b = 0; b < 2; ++b) {
        const float m0 = __uint_as_float(mt[2 * b].x), m1 = __uint_as_float(mt[2 * b + 1].x);
        Mb[b] = x32maxf(x16maxf(fmaxf(m0, m1)));
        const unsigned wp = pk2(__builtin_amdgcn_exp2f(m0 - Mb[b]), __builtin_amdgcn_exp2f(m1 - Mb[b]));
        const bool mine = (n >> 3) == b;
        acc = mfma(diag(dg, mine ? (wp & 0xffffu) : 0u), p[2 * b], acc); acc = mfma(diag(dg, mine ? (wp >> 16) : 0u), p[2 * b + 1], acc);
        Lb[b] = x32addf(x16addf(bflo(wp) * __uint_as_float(mt[2 * b].y) + bfhi(wp) * __uint_as_float(mt[2 * b + 1].y)));
      }
      *(volatile LAS f32x4*)(L + L_CP + ((w * 2 + (g >> 1)) * 128 + 8 * n + 4 * (g & 1)) * 4) = acc;
      if (lane < 2) *(volatile LAS v2u*)(L + L_CS + (lane * 8 + w) * 8) = v2u{min(__float_as_uint(lane ? Mb[1] : Mb[0]), SENT - 1u), min(__float_as_uint(lane ? Lb[1] : Lb[0]), SENT - 1u)}; }
    { const int b = n >> 3; float fk[8];
      { v4u s[4]; unsigned gv;
        for (;;) {
#pragma unroll
          for (int k = 0; k < 4; ++k) s[k] = lds_v4(L, L_CS + b * 64 + k * 16);
          gv = *(volatile LAS unsigned*)(L + L_G + b * 4);
          if (__all(nos(s[0]) & nos(s[1]) & nos(s[2]) & nos(s[3]) & (gv != SENT))) break;
          __builtin_amdgcn_s_sleep(SPN);
        }
        asm volatile("" ::: "memory");
        float M = -INFINITY, Ls = 0.f;
#pragma unroll
        for (int k = 0; k < 4; ++k) M = fmaxf(M, fmaxf(__uint_as_float(s[k].x), __uint_as_float(s[k].z)));
#pragma unroll
        for (int k = 0; k < 4; ++k) { fk[2 * k] = __builtin_amdgcn_exp2f(__uint_as_float(s[k].x) - M); fk[2 * k + 1] = __builtin_amdgcn_exp2f(__uint_as_float(s[k].z) - M);
          Ls = fmaf(fk[2 * k], __uint_as_float(s[k].y), fmaf(fk[2 * k + 1], __uint_as_float(s[k].w), Ls)); }
        const float sg = __uint_as_float(gv) * __builtin_amdgcn_rcpf(Ls);
        f32x2 c[16];
#pragma unroll
        for (int t = 0; t < 2; ++t)
#pragma unroll
          for (int k = 0; k < 8; ++k) c[t * 8 + k] = *(const LAS f32x2*)(L + L_CP + ((k * 2 + b) * 128 + 16 * w + 8 * t + 2 * g) * 4);
        f32x4 a0 = z4, a1 = z4;
#pragma unroll
        for (int t = 0; t < 2; ++t) { float s0 = 0.f, s1 = 0.f;
#pragma unroll
          for (int k = 0; k < 8; ++k) { s0 = fmaf(fk[k], c[t * 8 + k].x, s0); s1 = fmaf(fk[k], c[t * 8 + k].y, s1); }
          const unsigned sp = pk2(s0 * sg, s1 * sg);
          a0 = mfma(diag(dg, sp & 0xffffu), uv[t], a0); a1 = mfma(diag(dg, sp >> 16), uv[t], a1); }
        f32x4 v = n < 8 ? a0 : a1;
#pragma unroll
        for (int k = 0; k < 4; ++k) v[k] += dppf<ROR8>(v[k]);
        if (n < 8) *(volatile LAS f32x4*)(L + L_XP + ((w * 2 + (g >> 1)) * 64 + 8 * n + 4 * (g & 1)) * 4) = v; } }
    flag_set(L, F_XP + 4 * w, lane);
    if (w < 2) {
      flag_wait<8, SPN>(L, F_XP);
      float s = 0.f;
#pragma unroll
      for (int k = 0; k < 8; ++k) s += *(const LAS float*)(L + L_XP + ((k * 2 + w) * 64 + lane) * 4);
      *(volatile LAS unsigned*)(L + L_CTX + (w * 64 + (lane & 3) * 16 + (lane >> 2)) * 4) = min(__float_as_uint(s), SENT - 1u);
    }
    STAMP(6);
    if (w >= 2) {
      const int t6 = tid - 128, f = t6 >> 2, pt = t6 & 3, off = ((f / 48) * MD + 12 * (f % 48) + 4 * (pt < 3 ? pt : 0)) * 4;
      __builtin_amdgcn_s_setprio(0);
      flag_wait<7, 1>(L, F_OUT);
      __builtin_amdgcn_s_setprio(3);
      const f32x4 s = *(const LAS f32x4*)(L + L_OT + off) + *(const LAS f32x4*)(L + L_OF + off);
      v4u o = __builtin_bit_cast(v4u, s);
      unsigned U = 0, V = 0; cs(o, U, V); if (pt == 3) { U = elo; V = ehi; }
      U += dppu<XOR1>(U); U += dppu<XOR2>(U); V += dppu<XOR1>(V); V += dppu<XOR2>(V);
      if (pt == 3) o = v4u{U, V, 0u, 0u};
      ST128(o, WS_FO + f * 16384 + wg * 64 + pt * 16);
    }
    STAMP(7);
    if (wg < 96) {
      const int pt = lane & 3;
      v4u v0, v1; unsigned spins = 0;
      if (w < 2) flag_wait<7, 2>(L, F_OUT);
      __builtin_amdgcn_s_sleep(SLH3);
      for (;;) {
        asm volatile("" ::: "memory");
        v0 = LD128(WS_FO + wg * 16384 + tid * 16); v1 = LD128(WS_FO + wg * 16384 + 8192 + tid * 16);
        unsigned U = 0, V = 0; cs(v0, U, V); cs(v1, U, V);
        if (pt == 3) { U = 2u * elo - v0.x - v1.x; V = 2u * ehi - v0.y - v1.y; }
        if ((wsum(U) | wsum(V)) == 0u || ++spins > SPIN_CAP) break;
        __builtin_amdgcn_s_sleep(4);
      }
      f32x4 s = __builtin_bit_cast(f32x4, v0) + __builtin_bit_cast(f32x4, v1);
#pragma unroll
      for (int k = 0; k < 4; ++k) { float t = s[k]; t += dppf<ROR4>(t); t += dppf<ROR8>(t); t = x32addf(x16addf(t)); s[k] = t; }
      if (lane < 3) *(volatile LAS f32x4*)(L + L_FIN + w * 64 + lane * 16) = s;
      flag_set(L, F_FIN + 4 * w, lane);
      STAMP(11);
      if (w == 0) {
        const int b = wg / 48, col = 12 * (wg % 48) + (lane < 12 ? lane : 0);
        const float bb = (1.f - *(const LAS float*)(L + L_G + b * 4)) * bf1(a.b2[col]);
        flag_wait<8, SPN>(L, F_FIN);
        if (lane < 12) {
          float t = bb;
#pragma unroll
          for (int k = 0; k < 8; ++k) t += *(const LAS float*)(L + L_FIN + k * 64 + lane * 4);
          a.out[b * MD + col] = (u16)bfr(t);
          if (wg == 0 && lane == 0) __hip_atomic_store((GAS u64*)(a.ws + WS_EPOCH), ep, __ATOMIC_RELAXED, __HIP_MEMORY_SCOPE_AGENT);
        }
        STAMP(12);
      }
    }
  } else if (wave < 10) {
    const int kw = wave - 8;
    __builtin_amdgcn_s_setprio(3);
    const u64 ep = __hip_atomic_load((const GAS u64*)(a.ws + WS_EPOCH), __ATOMIC_RELAXED, __HIP_MEMORY_SCOPE_AGENT) + 1ull;
    const unsigned elo = (unsigned)ep, ehi = (unsigned)(ep >> 32);
    { v4u uk[8];
#pragma unroll
      for (int rg = 0; rg < 8; ++rg) uk[rg] = ldnt(a.wuk + ((size_t)(128 * cq + 64 * kw + 8 * (n & 7) + rg) * 32 + h) * 128 + 64 * dh + 32 * (n >> 3) + 8 * g);
      __builtin_amdgcn_sched_barrier(0);
      cnt_add(L, F_ISS, lane);
      STAMP(8);
      flag_wait<8, SPN>(L, F_QP);
      STAMP(10);
      { f32x2 s = {0.f, 0.f};
#pragma unroll
        for (int k = 0; k < 8; ++k) s += *(const LAS f32x2*)(L + L_QP + ((k * 2 + (lane >> 5)) * 64 + 2 * (lane & 31)) * 4);
        *(volatile LAS unsigned*)(L + L_QB + kw * 256 + lane * 4) = pk2(s.x, s.y); }
      v4u qa0 = lds_v4(L, L_QB + kw * 256 + (n & 1) * 128 + g * 16), qa1 = lds_v4(L, L_QB + kw * 256 + (n & 1) * 128 + 64 + g * 16);
      if (n >= 2) { qa0 = v4u{0u, 0u, 0u, 0u}; qa1 = qa0; }
      constexpr float SC = 0.08838834764831845f * 1.4426950408889634f;
      float sel[8];
#pragma unroll
      for (int rg = 0; rg < 8; ++rg) {
        const f32x4 a1 = mfma(qa0, uk[rg], z4), a2 = mfma(qa1, uk[rg], z4);
        float v0 = n < 8 ? a1.x : a2.x, v1 = n < 8 ? a1.y : a2.y;
        v0 += dppf<ROR8>(v0); v1 += dppf<ROR8>(v1);
        sel[rg] = (n < 8 ? v0 : v1) * SC;
      }
      const v4u o = {pk2(sel[0], sel[1]), pk2(sel[2], sel[3]), pk2(sel[4], sel[5]), pk2(sel[6], sel[7])};
      const int qb = WS_QA + (((n >> 3) * 32 + h) * 8 + 2 * cq + kw) * 384;
      if (lane < 16) ST128(o, qb + dh * 128 + (n & 7) * 16);
      unsigned U = 0, V = 0; cs(o, U, V);
      U += dppu<XOR1>(U); U += dppu<XOR2>(U); U += dppu<HMIR>(U); V += dppu<XOR1>(V); V += dppu<XOR2>(V); V += dppu<HMIR>(V);
      if (lane < 16 && (n & 7) == 0) ST64((v2u{U + elo, V + ehi}), qb + 256 + dh * 8);
      cnt_add(L, F_SENT, lane); }
    __builtin_amdgcn_s_setprio(0);
    STAMP(1);
    cnt_wait<4>(L, F_PUB, 8u);
    f32x4 acc[3] = {z4, z4, z4};
#pragma unroll
    for (int pass = 0; pass < 2; ++pass) {
      const int it = wg + 256 * pass;
      if (pass == 1 && wg >= 32) break;
      const int hb = L_HID + pass * 128;
      v4u w2v[12];
      const u16* wp = a.w2 + (size_t)(16 * it + g) * MD + 384 * kw;
#pragma unroll
      for (int t = 0; t < 4; ++t) { w2v[t] = ldnt(wp + (size_t)4 * t * MD + 8 * n); w2v[4 + t] = ldnt(wp + (size_t)4 * t * MD + 128 + 8 * (kw ? (n & 7) : n)); }
      if (kw == 0) {
#pragma unroll
        for (int t = 0; t < 4; ++t) w2v[8 + t] = ldnt(wp + (size_t)4 * t * MD + 256 + 8 * n);
        const v2u b1p = *(const v2u*)(a.b1 + 16 * it + (n & 1) * 8 + 4 * (g & 1));
        v4u v[6]; unsigned spins = 0;
        for (;;) {
          asm volatile("" ::: "memory");
#pragma unroll
          for (int k = 0; k < 6; ++k) v[k] = LD128(WS_HX + ((it >> 3) * 6 + k) * 1152 + lane * 16);
          const v2u mt = LD64(WS_HX + ((it >> 3) * 6 + (lane < 6 ? lane : 0)) * 1152 + 1024);
          unsigned U = 0, V = 0;
#pragma unroll
          for (int k = 0; k < 6; ++k) cs(v[k], U, V);
          if (lane < 6) { U -= mt.x - elo; V -= mt.y - ehi; }
          if ((wsum(U) | wsum(V)) == 0u || ++spins > SPIN_CAP) break;
          __builtin_amdgcn_s_sleep(20);
        }
        unsigned gv; do { gv = *(volatile LAS unsigned*)(L + L_G + (g >> 1) * 4); } while (!__all(gv != SENT));
        f32x4 s = z4;
#pragma unroll
        for (int k = 0; k < 6; ++k) s += __builtin_bit_cast(f32x4, v[k]);
        if ((n >> 1) == (it & 7)) {
          const float og = 1.f - __uint_as_float(gv);
          f32x4 hv; hv.x = fmaxf(s.x + bflo(b1p.x), 0.f); hv.y = fmaxf(s.y + bfhi(b1p.x), 0.f); hv.z = fmaxf(s.z + bflo(b1p.y), 0.f); hv.w = fmaxf(s.w + bfhi(b1p.y), 0.f);
          *(volatile LAS f32x4*)(L + hb + ((g >> 1) * 16 + 8 * (n & 1) + 4 * (g & 1)) * 4) = hv * hv * og;
        }
        cnt_add(L, F_HID, lane);
      } else cnt_wait<4>(L, F_HID, (unsigned)pass + 1u);
#pragma unroll
      for (int t = 0; t < 4; ++t) { const v4u d = diag(dg, bfr(*(volatile LAS float*)(L + hb + ((n >> 3) * 16 + 4 * t + g) * 4)));
        acc[0] = mfma(d, w2v[t], acc[0]); acc[1] = mfma(d, w2v[4 + t], acc[1]); if (kw == 0) acc[2] = mfma(d, w2v[8 + t], acc[2]); }
    }
    { const int ob = L_OF + ((g >> 1) * MD + 384 * kw + 8 * n + 4 * (g & 1)) * 4;
      *(volatile LAS f32x4*)(L + ob) = acc[0];
      if (kw == 0 || n < 8) *(volatile LAS f32x4*)(L + ob + 512) = acc[1];
      if (kw == 0) *(volatile LAS f32x4*)(L + ob + 1024) = acc[2]; }
    flag_set(L, F_OUT + 4 * (5 + kw), lane);
    STAMP(3);
  } else if (wave < 15) {
    const int T = wave - 10;
    const u16* p = a.wo + (size_t)(h * 128 + 64 * dh) * MD;
    if (T < 4) {
      cnt_wait<1>(L, F_ISS, 10u);
      { const unsigned char* src = (const unsigned char*)(a.ckv + (size_t)kb * 128 * 512);
#pragma unroll 1
        for (int i = T; i < 128; i += 4) {
          const int row = (i + 64 * hgA) & 127;
          const int f = ((row & 3) << 2) | ((row >> 2) & 3);
          glds16_nt(src + row * 1024 + ((lane ^ f) * 16), (unsigned)(size_t)(L + L_KV) + row * 1024);
        } }
      cnt_add(L, F_DMAI, lane);
      STAMP(8);
      VMWAIT(0);
      flag_set(L, F_KV + 4 * T, lane);
      STAMP(1);
      cnt_wait<2>(L, F_GO, 8u);
      v4u wo[16];
#pragma unroll
      for (int t = 0; t < 16; ++t) {
        if (t == ST1) { __builtin_amdgcn_sched_barrier(0); cnt_wait<3>(L, F_PUB, 8u); }
        if (t == ST2) { __builtin_amdgcn_sched_barrier(0); cnt_wait<3>(L, F_H2, 8u); }
        wo[t] = ldnt(p + (size_t)(4 * ((t + 4 * cq) & 15) + g) * MD + 128 * T + 8 * n); }
      __builtin_amdgcn_sched_barrier(0);
      STAMP(2);
      while (*(volatile LAS unsigned*)(L + F_XP) == SENT) __builtin_amdgcn_s_sleep(4);
      __builtin_amdgcn_s_setprio(3);
      f32x4 acc = z4; v4u c[4];
      for (;;) {
#pragma unroll
        for (int t4 = 0; t4 < 4; ++t4) c[t4] = lds_v4(L, L_CTX + ((n >> 3) * 64 + g * 16 + 4 * ((t4 + cq) & 3)) * 4);
        if (__all(nos(c[0]) & nos(c[1]) & nos(c[2]) & nos(c[3]))) break;
        __builtin_amdgcn_s_sleep(SPN);
      }
#pragma unroll
      for (int t4 = 0; t4 < 4; ++t4) { const unsigned c01 = pk2(__uint_as_float(c[t4].x), __uint_as_float(c[t4].y)), c23 = pk2(__uint_as_float(c[t4].z), __uint_as_float(c[t4].w));
        acc = mfma(diag(dg, c01 & 0xffffu), wo[4 * t4], acc); acc = mfma(diag(dg, c01 >> 16), wo[4 * t4 + 1], acc);
        acc = mfma(diag(dg, c23 & 0xffffu), wo[4 * t4 + 2], acc); acc = mfma(diag(dg, c23 >> 16), wo[4 * t4 + 3], acc); }
      *(volatile LAS f32x4*)(L + L_OT + ((g >> 1) * MD + 128 * T + 8 * n + 4 * (g & 1)) * 4) = acc;
    } else {
      cnt_wait<4>(L, F_PUB, 8u);
      v4u wo[8];
#pragma unroll
      for (int t = 0; t < 8; ++t) wo[t] = ldnt(p + (size_t)(8 * t + 2 * g + (n >> 3)) * MD + 512 + 8 * (n & 7));
      __builtin_amdgcn_sched_barrier(0);
      while (*(volatile LAS unsigned*)(L + F_XP) == SENT) __builtin_amdgcn_s_sleep(4);
      __builtin_amdgcn_s_setprio(3);
      v4u c[8];
      for (;;) {
#pragma unroll
        for (int k = 0; k < 4; ++k) { c[k] = lds_v4(L, L_CTX + ((n >> 3) * 64 + (2 * (g & 1)) * 16 + 4 * k) * 4); c[4 + k] = lds_v4(L, L_CTX + ((n >> 3) * 64 + (2 * (g & 1) + 1) * 16 + 4 * k) * 4); }
        if (__all(nos(c[0]) & nos(c[1]) & nos(c[2]) & nos(c[3]) & nos(c[4]) & nos(c[5]) & nos(c[6]) & nos(c[7]))) break;
        __builtin_amdgcn_s_sleep(SPN);
      }
      f32x4 a0 = z4, a1 = z4;
#pragma unroll
      for (int t = 0; t < 8; ++t) { const int e = 2 * (t & 1);
        const unsigned u0 = (g >> 1) ? c[t >> 1][e + 1] : c[t >> 1][e], u1 = (g >> 1) ? c[4 + (t >> 1)][e + 1] : c[4 + (t >> 1)][e];
        const unsigned cp = pk2(__uint_as_float(u0), __uint_as_float(u1));
        a0 = mfma(diag(dg, cp & 0xffffu), wo[t], a0); a1 = mfma(diag(dg, cp >> 16), wo[t], a1); }
      f32x4 v = n < 8 ? a0 : a1;
#pragma unroll
      for (int k = 0; k < 4; ++k) v[k] += dppf<ROR8>(v[k]);
      if (n < 8) *(volatile LAS f32x4*)(L + L_OT + ((g >> 1) * MD + 512 + 8 * n + 4 * (g & 1)) * 4) = v;
    }
    flag_set(L, F_OUT + 4 * T, lane);
    STAMP(3);
  } else {
    asm volatile("s_waitcnt vmcnt(0)" ::: "memory");
    cnt_add(L, F_X, lane);
    const u64 ep = __hip_atomic_load((const GAS u64*)(a.ws + WS_EPOCH), __ATOMIC_RELAXED, __HIP_MEMORY_SCOPE_AGENT) + 1ull;
    const unsigned elo = (unsigned)ep, ehi = (unsigned)(ep >> 32);
    cnt_wait<1>(L, F_ISS, 10u);
    { float s0 = 0.f, s1 = 0.f;
      if (lane < 36) {
        const v4u gw = *(const v4u*)(a.gw + 8 * lane), gw2 = *(const v4u*)(a.gw + 288 + 8 * lane);
        const v4u xa = *(const LAS v4u*)(L + L_X + 16 * lane), xa2 = *(const LAS v4u*)(L + L_X + 576 + 16 * lane), xb = *(const LAS v4u*)(L + L_X + 1152 + 16 * lane), xb2 = *(const LAS v4u*)(L + L_X + 1728 + 16 * lane);
#pragma unroll
        for (int k = 0; k < 4; ++k) {
          s0 += bflo(gw[k]) * bflo(xa[k]) + bfhi(gw[k]) * bfhi(xa[k]) + bflo(gw2[k]) * bflo(xa2[k]) + bfhi(gw2[k]) * bfhi(xa2[k]);
          s1 += bflo(gw[k]) * bflo(xb[k]) + bfhi(gw[k]) * bfhi(xb[k]) + bflo(gw2[k]) * bflo(xb2[k]) + bfhi(gw2[k]) * bfhi(xb2[k]); }
      }
      s0 = wsumf(s0); s1 = wsumf(s1);
      if (lane < 2) *(volatile LAS unsigned*)(L + L_G + lane * 4) = min(__float_as_uint(1.f / (1.f + __expf(-(lane ? s1 : s0)))), SENT - 1u); }
    if (wg < 216) {
      const int T = wg / 6, rg = wg % 6;
      cnt_wait<4>(L, F_DMAI, 4u);
      const u16* p = a.w1 + (size_t)(96 * rg + 24 * g) * NF + 128 * T + 8 * n;
      v4u wv[24];
#pragma unroll
      for (int t = 0; t < 24; ++t) wv[t] = ldnt(p + (size_t)t * NF);
      __builtin_amdgcn_sched_barrier(0);
      v4u xs[3];
#pragma unroll
      for (int k = 0; k < 3; ++k) xs[k] = *(const LAS v4u*)(L + L_X + (n >> 3) * 1152 + (96 * rg + 24 * g) * 2 + 16 * k);
      f32x4 acc = z4;
#pragma unroll
      for (int t = 0; t < 24; ++t) { const unsigned xd = xs[t >> 3][(t >> 1) & 3]; acc = mfma(diag(dg, (t & 1) ? (xd >> 16) : (xd & 0xffffu)), wv[t], acc); }
      const v4u o = __builtin_bit_cast(v4u, acc);
      ST128(o, WS_HX + wg * 1152 + lane * 16);
      unsigned U = 0, V = 0; cs(o, U, V); U = wsum(U); V = wsum(V);
      if (lane == 0) ST64((v2u{U + elo, V + ehi}), WS_HX + wg * 1152 + 1024);
      STAMP(3);
    }
  }
  STAMP_OUT();
}
}

extern "C" void kernel_launch(void* const* d_in, const int* in_sizes, int n_in, void* d_out, int out_size, void* d_ws, size_t ws_size, hipStream_t stream) {
  Args a;
  a.x = (const u16*)d_in[0]; a.ckv = (const u16*)d_in[1]; a.wq = (const u16*)d_in[2]; a.wuk = (const u16*)d_in[3]; a.wuv = (const u16*)d_in[4]; a.wo = (const u16*)d_in[5];
  a.w1 = (const u16*)d_in[7]; a.b1 = (const u16*)d_in[8]; a.w2 = (const u16*)d_in[9]; a.b2 = (const u16*)d_in[10]; a.gw = (const u16*)d_in[11];
  a.out = (u16*)d_out; a.ws = (unsigned char*)d_ws;
  (void)hipFuncSetAttribute((const void*)fused_kernel, hipFuncAttributeMaxDynamicSharedMemorySize, L_END);
  hipLaunchKernelGGL(fused_kernel, dim3(256), dim3(1024), L_END, stream, a);
}
```

```cpp
#define PROBE 0
#include <hip/hip_runtime.h>
#include <hip/hip_bf16.h>
#include <stdint.h>

typedef unsigned short u16;
typedef unsigned long long u64;
typedef unsigned v4u __attribute__((ext_vector_type(4)));
typedef unsigned v2u __attribute__((ext_vector_type(2)));
typedef float f32x4 __attribute__((ext_vector_type(4)));
typedef float f32x2 __attribute__((ext_vector_type(2)));
typedef short bf16x8 __attribute__((ext_vector_type(8)));
typedef short s16x4 __attribute__((ext_vector_type(4)));
#define LAS __attribute__((address_space(3)))
#define GAS __attribute__((address_space(1)))
#define DI __device__ __forceinline__

namespace {
constexpr int MD = 576, NF = 4608;
constexpr int WS_EPOCH = 0;
constexpr int WS_QA = 4096;
constexpr int WS_HX = WS_QA + 512 * 384;
constexpr int WS_PM = WS_HX + 216 * 1152;
constexpr int WS_PO = WS_PM + 128 * 8192;
constexpr int WS_FO = WS_PO + 128 * 32768;
constexpr int WS_END = WS_FO + 96 * 16384;
constexpr int WS_STAMP = WS_END + 4096;
constexpr int L_KV = 0;
constexpr int L_QA = 131072;
constexpr int L_CP = L_QA, L_XP = L_QA + 8192;
constexpr int L_P = L_QA + 16384;
constexpr int L_QP = L_P;
constexpr int L_OT = L_KV;
constexpr int L_OF = L_P + 4096;
constexpr int L_X = L_OF + 4608;
constexpr int L_QB = L_X + 3072;
constexpr int L_HID = L_QB + 512;
constexpr int L_S0 = L_HID + 256;
constexpr int L_SM = L_S0;
constexpr int L_SS = L_SM + 512;
constexpr int L_CS = L_SS + 512;
constexpr int L_CTX = L_CS + 128;
constexpr int L_G = L_CTX + 512;
constexpr int L_FL = L_G + 16;
constexpr int F_QP = L_FL, F_QA = L_FL + 32, F_KV = L_FL + 64, F_XP = L_FL + 96, F_OUT = L_FL + 128, F_FIN = L_FL + 160, F_MISC = L_FL + 192;
constexpr int F_X = F_MISC, F_ISS = F_MISC + 4, F_SENT = F_MISC + 8, F_DMAI = F_MISC + 12, F_HID = F_MISC + 16, F_GO = F_MISC + 20, F_PUB = F_MISC + 24, F_H2 = F_MISC + 28;
constexpr int L_S1 = L_FL + 224;
constexpr int L_FIN = L_S1;
constexpr int L_PR = L_FIN + 512;
constexpr int L_END = L_PR + 256;
static_assert(L_END <= 163840 && (L_S1 - L_S0) % 16 == 0 && (L_S1 - L_S0) / 16 <= 128, "lds");
#define SPINCAP (1u << 18)
#define GOEARLY 1
#define SPN 1
#define SLT 6
#define ST1 6
#define ST2 11
#define SLH1 4
#define SLH2 24
#define SLH3 16
constexpr unsigned SPIN_CAP = SPINCAP, SENT = 0xFFFFFFFFu;
constexpr int AUX_SC1 = 16;

struct Args {
  const u16 *x, *ckv, *wq, *wuk, *wuv, *wo, *w1, *b1, *w2, *b2, *gw;
  u16* out;
  unsigned char* ws;
};

DI void bar() { asm volatile("s_waitcnt lgkmcnt(0)" ::: "memory"); __builtin_amdgcn_s_barrier(); asm volatile("" ::: "memory"); }
DI void lds_wait(LAS unsigned char* L, int off, unsigned want) {
  while (__hip_atomic_load((LAS unsigned*)(L + off), __ATOMIC_RELAXED, __HIP_MEMORY_SCOPE_WORKGROUP) < want) __builtin_amdgcn_s_sleep(1);
  asm volatile("" ::: "memory");
}
DI void lds_wait_fast(LAS unsigned char* L, int off, unsigned want) {
  while (__hip_atomic_load((LAS unsigned*)(L + off), __ATOMIC_RELAXED, __HIP_MEMORY_SCOPE_WORKGROUP) < want) { }
  asm volatile("" ::: "memory");
}
DI void lds_wait_slow(LAS unsigned char* L, int off, unsigned want) {
  while (__hip_atomic_load((LAS unsigned*)(L + off), __ATOMIC_RELAXED, __HIP_MEMORY_SCOPE_WORKGROUP) < want) __builtin_amdgcn_s_sleep(4);
  asm volatile("" ::: "memory");
}
DI void lds_set(LAS unsigned char* L, int off, int lane) {
  asm volatile("s_waitcnt lgkmcnt(0)" ::: "memory");
  if (lane == 0) (void)__hip_atomic_fetch_add((LAS unsigned*)(L + off), 1u, __ATOMIC_RELAXED, __HIP_MEMORY_SCOPE_WORKGROUP);
}
DI v4u ldnt(const void* p) { return *(const GAS v4u*)p; }
DI f32x4 mfma(v4u a, v4u b, f32x4 c) { return __builtin_amdgcn_mfma_f32_16x16x32_bf16(__builtin_bit_cast(bf16x8, a), __builtin_bit_cast(bf16x8, b), c, 0, 0, 0); }
DI float bflo(unsigned u) { return __uint_as_float(u << 16); }
DI float bfhi(unsigned u) { return __uint_as_float(u & 0xffff0000u); }
DI float bf1(u16 v) { return __uint_as_float(((unsigned)v) << 16); }
DI unsigned pk2(float lo, float hi) {
  typedef __bf16 bf2 __attribute__((ext_vector_type(2)));
  bf2 r; r.x = (__bf16)lo; r.y = (__bf16)hi; return __builtin_bit_cast(unsigned, r);
}
DI unsigned rotl(unsigned x, unsigned r) { return __builtin_amdgcn_alignbit(x, x, 32u - r); }
template <int CTRL> DI unsigned dppu(unsigned x) { return (unsigned)__builtin_amdgcn_mov_dpp((int)x, CTRL, 0xf, 0xf, true); }
template <int CTRL> DI float dppf(float x) { return __uint_as_float(dppu<CTRL>(__float_as_uint(x))); }
constexpr int XOR1 = 0xB1, XOR2 = 0x4E, HMIR = 0x141, ROR4 = 0x124, ROR8 = 0x128;
DI unsigned x16addu(unsigned x) { auto s = __builtin_amdgcn_permlane16_swap(x, x, false, false); return s[0] + s[1]; }
DI unsigned x32addu(unsigned x) { auto s = __builtin_amdgcn_permlane32_swap(x, x, false, false); return s[0] + s[1]; }
DI float x16addf(float x) { auto s = __builtin_amdgcn_permlane16_swap(__float_as_uint(x), __float_as_uint(x), false, false); return __uint_as_float(s[0]) + __uint_as_float(s[1]); }
DI float x32addf(float x) { auto s = __builtin_amdgcn_permlane32_swap(__float_as_uint(x), __float_as_uint(x), false, false); return __uint_as_float(s[0]) + __uint_as_float(s[1]); }
DI float x16maxf(float x) { auto s = __builtin_amdgcn_permlane16_swap(__float_as_uint(x), __float_as_uint(x), false, false); return fmaxf(__uint_as_float(s[0]), __uint_as_float(s[1])); }
DI float x32maxf(float x) { auto s = __builtin_amdgcn_permlane32_swap(__float_as_uint(x), __float_as_uint(x), false, false); return fmaxf(__uint_as_float(s[0]), __uint_as_float(s[1])); }
DI unsigned wsum(unsigned x) { x += dppu<XOR1>(x); x += dppu<XOR2>(x); x += dppu<ROR4>(x); x += dppu<ROR8>(x); return x32addu(x16addu(x)); }
DI float wsumf(float x) { x += dppf<XOR1>(x); x += dppf<XOR2>(x); x += dppf<ROR4>(x); x += dppf<ROR8>(x); return x32addf(x16addf(x)); }
DI unsigned d2(unsigned a, unsigned k, unsigned c) { typedef unsigned short us2 __attribute__((ext_vector_type(2)));
  return __builtin_amdgcn_udot2(__builtin_bit_cast(us2, a), __builtin_bit_cast(us2, k), c, false); }
DI void cs(v4u a, unsigned& U, unsigned& V) {
  U = d2(a.x, 0x9E37B5C5u, U); U = d2(a.y, 0x7A4DC2B3u, U); U = d2(a.z, 0xE4F1594Du, U); U = d2(a.w, 0x3B8FD16Fu, U);
  V = d2(a.x, 0x62E9F2A7u, V); V = d2(a.y, 0xC96B4F1Du, V); V = d2(a.z, 0x5573A9E5u, V); V = d2(a.w, 0xED2F8799u, V);
}
DI v4u lds_v4(LAS unsigned char* L, int off) { return *(volatile LAS v4u*)(L + off); }
DI bool nos(v4u v) { return (v.x != SENT) & (v.y != SENT) & (v.z != SENT) & (v.w != SENT); }
template <int N, int SLEEP> DI void flag_wait(LAS unsigned char* L, int off) {
  for (;;) { bool ok = nos(lds_v4(L, off)); if (N > 4) { const v4u b = lds_v4(L, off + 16); ok &= (b.x != SENT) & (N < 6 || b.y != SENT) & (N < 7 || b.z != SENT) & (N < 8 || b.w != SENT); }
    if (ok) break; if (SLEEP) __builtin_amdgcn_s_sleep(SLEEP); }
  asm volatile("" ::: "memory");
}
DI void flag_set(LAS unsigned char* L, int off, int lane) { asm volatile("" ::: "memory"); if (lane == 0) *(volatile LAS unsigned*)(L + off) = 1u; asm volatile("" ::: "memory"); }
template <int SLEEP> DI void cnt_wait(LAS unsigned char* L, int off, unsigned n) {
  while (*(volatile LAS unsigned*)(L + off) + 1u < n) { if (SLEEP) __builtin_amdgcn_s_sleep(SLEEP); }
  asm volatile("" ::: "memory");
}
DI void cnt_add(LAS unsigned char* L, int off, int lane) { asm volatile("" ::: "memory"); if (lane == 0) (void)__hip_atomic_fetch_add((LAS unsigned*)(L + off), 1u, __ATOMIC_RELAXED, __HIP_MEMORY_SCOPE_WORKGROUP); asm volatile("" ::: "memory"); }
struct Diag { unsigned m0, m1, m2, m3, sh; };
DI Diag make_diag(int i) { const int p = i & 7; Diag d; d.sh = (p & 1) * 16; d.m0 = (p >> 1) == 0 ? ~0u : 0u; d.m1 = (p >> 1) == 1 ? ~0u : 0u; d.m2 = (p >> 1) == 2 ? ~0u : 0u; d.m3 = (p >> 1) == 3 ? ~0u : 0u; return d; }
DI v4u diag(const Diag& d, unsigned vb16) { const unsigned v = vb16 << d.sh; return v4u{v & d.m0, v & d.m1, v & d.m2, v & d.m3}; }
DI unsigned bfr(float f) { return pk2(f, 0.f) & 0xffffu; }
DI void glds16_nt(const void* gsrc, unsigned lds_dst) {
  unsigned keep;
  asm volatile("s_mov_b32 %0, m0\n\ts_mov_b32 m0, %2\n\ts_nop 0\n\tglobal_load_lds_dwordx4 %1, off\n\ts_mov_b32 m0, %0" : "=&s"(keep) : "v"(gsrc), "s"(lds_dst) : "memory");
}
#define LD128(off) __builtin_amdgcn_raw_buffer_load_b128(rs, (off), 0, AUX_SC1)
#define LD64(off) __builtin_amdgcn_raw_buffer_load_b64(rs, (off), 0, AUX_SC1)
#define ST128(v, off) __builtin_amdgcn_raw_buffer_store_b128((v), rs, (off), 0, AUX_SC1)
#define ST64(v, off) __builtin_amdgcn_raw_buffer_store_b64((v), rs, (off), 0, AUX_SC1)
#define VMWAIT(N) do { __builtin_amdgcn_sched_barrier(0); asm volatile("s_waitcnt vmcnt(%0)" :: "n"(N) : "memory"); __builtin_amdgcn_sched_barrier(0); } while (0)
#define STAMP(k) do { if (wg == 0 && (wave == 0 || wave == 8 || wave == 10) && lane == 0) *(volatile LAS unsigned*)(L + L_PR + ((wave == 0 ? 0 : wave == 8 ? 16 : 32) + (k)) * 4) = (unsigned)__builtin_amdgcn_s_memrealtime(); } while (0)
#define STAMP_OUT() do { } while (0)

__global__ void __launch_bounds__(1024) fused_kernel(Args a) {
  extern __shared__ __attribute__((aligned(16))) unsigned char smem[];
  LAS unsigned char* L = (LAS unsigned char*)smem;
  const int tid = threadIdx.x, lane = tid & 63, wave = __builtin_amdgcn_readfirstlane(tid >> 6);
  const int wg = blockIdx.x, xl = wg & 7, r = wg >> 3;
  const int n = lane & 15, g = lane >> 4;
  const int h = xl + 8 * (r >> 3), j = r & 7, cq = j >> 1, dh = j & 1;
  const int hgA = r >> 4, kb = xl * 16 + (r & 15), cb = kb >> 6;
  const __amdgpu_buffer_rsrc_t rs = __builtin_amdgcn_make_buffer_rsrc(a.ws, 0, WS_END, 0x00020000);
  const Diag dg = make_diag(n);
  const f32x4 z4 = {0.f, 0.f, 0.f, 0.f};
  STAMP(0);
  if (wave == 15) {
    const unsigned char* xs = (const unsigned char*)a.x;
    glds16_nt(xs + lane * 16, (unsigned)(size_t)(L + L_X)); glds16_nt(xs + 1024 + lane * 16, (unsigned)(size_t)(L + L_X) + 1024);
    glds16_nt(xs + 2048 + (lane < 16 ? lane : 15) * 16, (unsigned)(size_t)(L + L_X) + 2048); }
  if (tid < (L_S1 - L_S0) / 16) *(LAS v4u*)(L + L_S0 + tid * 16) = v4u{SENT, SENT, SENT, SENT};
  bar();

  if (wave < 8) {
    const int w = wave;
    __builtin_amdgcn_s_setprio(3);
    { v4u wv[9];
      const u16* p = a.wq + (size_t)(2 * g + (n >> 3)) * 4096 + h * 128 + dh * 64 + 8 * (n & 7);
#pragma unroll
      for (int k = 0; k < 9; ++k) { int I = w + 8 * k + 18 * cq; I = I >= 72 ? I - 72 : I; wv[k] = ldnt(p + (size_t)I * (8 * 4096)); }
      __builtin_amdgcn_sched_barrier(0);
      cnt_add(L, F_ISS, lane);
      STAMP(8);
      cnt_wait<1>(L, F_X, 1u);
      unsigned xv[9];
#pragma unroll
      for (int k = 0; k < 9; ++k) { int I = w + 8 * k + 18 * cq; I = I >= 72 ? I - 72 : I; xv[k] = *(const LAS unsigned*)(L + L_X + (n >> 3) * 1152 + (8 * I + 2 * g) * 2); }
      f32x4 a0 = z4, a1 = z4;
#pragma unroll
      for (int k = 0; k < 9; ++k) { a0 = mfma(diag(dg, xv[k] & 0xffffu), wv[k], a0); a1 = mfma(diag(dg, xv[k] >> 16), wv[k], a1); }
      f32x4 v = n < 8 ? a0 : a1;
#pragma unroll
      for (int k = 0; k < 4; ++k) v[k] += dppf<ROR8>(v[k]);
      if (n < 8) *(LAS f32x4*)(L + L_QP + ((w * 2 + (g >> 1)) * 64 + 8 * n + 4 * (g & 1)) * 4) = v;
      flag_set(L, F_QP + 4 * w, lane); }
    STAMP(1);
    const u64 ep = __hip_atomic_load((const GAS u64*)(a.ws + WS_EPOCH), __ATOMIC_RELAXED, __HIP_MEMORY_SCOPE_AGENT) + 1ull;
    const unsigned elo = (unsigned)ep, ehi = (unsigned)(ep >> 32);
    __builtin_amdgcn_s_setprio(0);
    cnt_wait<2>(L, F_SENT, 2u);
    flag_wait<4, 2>(L, F_KV);
    __builtin_amdgcn_s_setprio(3);
    v4u kf[16];
    { const int k8 = lane & 7, hq = lane >> 3;
      const int qb = WS_QA + ((cb * 32 + 16 * hgA + hq) * 8 + w) * 384 + k8 * 16, mo = WS_QA + ((cb * 32 + 16 * hgA + n) * 8 + w) * 384 + 256;
      v4u v0, v1, v2, v3; unsigned spins = 0;
      for (;;) {
        asm volatile("" ::: "memory");
        v0 = LD128(qb); v1 = LD128(qb + 128); v2 = LD128(qb + 8 * 3072); v3 = LD128(qb + 8 * 3072 + 128);
        const v4u mt = LD128(mo);
        __builtin_amdgcn_sched_barrier(0);
        if (spins == 0) {
          cnt_add(L, F_GO, lane);
          const int f = ((n & 3) << 2) | (n >> 2);
          const int rowb = L_KV + (16 * w + n) * 1024;
#pragma unroll
          for (int s = 0; s < 16; ++s) kf[s] = *(const LAS v4u*)(L + rowb + (s >> 2) * 256 + (((4 * g + (s & 3)) ^ f) * 16)); }
        __builtin_amdgcn_sched_barrier(0);
        unsigned U = 0, V = 0; cs(v0, U, V); cs(v1, U, V); cs(v2, U, V); cs(v3, U, V);
        if (lane < 16) { U -= mt.x + mt.z - 2u * elo; V -= mt.y + mt.w - 2u * ehi; }
        if ((wsum(U) | wsum(V)) == 0u || ++spins > SPIN_CAP) break;
        __builtin_amdgcn_s_sleep(6);
      }
      auto add2 = [](v4u p, v4u q) { v4u o;
        o.x = pk2(bflo(p.x) + bflo(q.x), bfhi(p.x) + bfhi(q.x)); o.y = pk2(bflo(p.y) + bflo(q.y), bfhi(p.y) + bfhi(q.y));
        o.z = pk2(bflo(p.z) + bflo(q.z), bfhi(p.z) + bfhi(q.z)); o.w = pk2(bflo(p.w) + bflo(q.w), bfhi(p.w) + bfhi(q.w)); return o; };
      const int wb = L_QA + (4 * (w >> 1) + (k8 & 3)) * 1024 + (2 * (w & 1) + (k8 >> 2)) * 256, rot = hq + 4 * (k8 & 3) + 2 * (w & 1);
      *(LAS v4u*)(L + wb + (rot & 15) * 16) = add2(v0, v1);
      *(LAS v4u*)(L + wb + ((rot + 8) & 15) * 16) = add2(v2, v3);
      flag_set(L, F_QA + 4 * w, lane);
    }
    STAMP(2);
    flag_wait<8, SPN>(L, F_QA);
    STAMP(3);
    f32x4 sc = z4, sc2 = z4;
#pragma unroll
    for (int hs = 0; hs < 2; ++hs) {
      v4u qf[8];
#pragma unroll
      for (int s = 0; s < 8; ++s) qf[s] = *(const LAS v4u*)(L + L_QA + (8 * hs + s) * 1024 + g * 256 + ((n + 4 * (s & 3) + 2 * (g >> 1)) & 15) * 16);
      __builtin_amdgcn_sched_barrier(0);
#pragma unroll
      for (int s = 0; s < 8; s += 2) { sc = mfma(kf[8 * hs + s], qf[s], sc); sc2 = mfma(kf[8 * hs + s + 1], qf[s + 1], sc2); }
    }
    sc += sc2;
    { float m = fmaxf(fmaxf(sc.x, sc.y), fmaxf(sc.z, sc.w)); m = x32maxf(x16maxf(m));
      if (g == 0) *(volatile LAS unsigned*)(L + L_SM + n * 32 + w * 4) = min(__float_as_uint(m), SENT - 1u); }
    float mcu;
    { v4u m0, m1;
      for (;;) { m0 = lds_v4(L, L_SM + n * 32); m1 = lds_v4(L, L_SM + n * 32 + 16); if (__all(nos(m0) & nos(m1))) break; __builtin_amdgcn_s_sleep(SPN); }
      STAMP(9);
      const f32x4 f0 = __builtin_bit_cast(f32x4, m0), f1 = __builtin_bit_cast(f32x4, m1);
      mcu = fmaxf(fmaxf(fmaxf(f0.x, f0.y), fmaxf(f0.z, f0.w)), fmaxf(fmaxf(f1.x, f1.y), fmaxf(f1.z, f1.w)));
      const unsigned p01 = pk2(__builtin_amdgcn_exp2f(sc.x - mcu), __builtin_amdgcn_exp2f(sc.y - mcu));
      const unsigned p23 = pk2(__builtin_amdgcn_exp2f(sc.z - mcu), __builtin_amdgcn_exp2f(sc.w - mcu));
      *(volatile LAS v2u*)(L + L_P + (w >> 1) * 1024 + (n + 16 * (2 * (w & 1) + (g >> 1))) * 16 + 8 * (g & 1)) = v2u{p01, p23};
      float l = (bflo(p01) + bfhi(p01)) + (bflo(p23) + bfhi(p23)); l = x32addf(x16addf(l));
      if (g == 0) *(volatile LAS unsigned*)(L + L_SS + n * 32 + w * 4) = min(__float_as_uint(l), SENT - 1u); }
    { const int q4 = n >> 2, p4 = n & 3;
      const unsigned base = L_KV + (8 * g + q4) * 1024 + (w >> 1) * 256 + ((((8 * (w & 1)) + (p4 >> 1)) ^ ((q4 << 2) | (2 * (g & 1)))) * 16) + 8 * (p4 & 1);
      bf16x8 vf[16];
#pragma unroll
      for (int ks = 0; ks < 4; ++ks)
#pragma unroll
        for (int T = 0; T < 4; ++T) {
          const s16x4 lo = __builtin_amdgcn_ds_read_tr16_b64_v4i16((LAS s16x4*)(L + ((base ^ (unsigned)(T << 5)) + ks * 32768)));
          const s16x4 hi = __builtin_amdgcn_ds_read_tr16_b64_v4i16((LAS s16x4*)(L + ((base ^ (unsigned)((T << 5) | (1 << 4) | (1 << 12))) + ks * 32768)));
          vf[ks * 4 + T] = __builtin_shufflevector(lo, hi, 0, 1, 2, 3, 4, 5, 6, 7); }
      __builtin_amdgcn_sched_barrier(0);
      v4u l0, l1;
      for (;;) { l0 = lds_v4(L, L_SS + n * 32); l1 = lds_v4(L, L_SS + n * 32 + 16); if (__all(nos(l0) & nos(l1))) break; __builtin_amdgcn_s_sleep(SPN); }
      asm volatile("" ::: "memory");
      STAMP(10);
      f32x4 o[4] = {z4, z4, z4, z4};
      v4u pf[4];
#pragma unroll
      for (int ks = 0; ks < 4; ++ks) pf[ks] = *(const LAS v4u*)(L + L_P + ks * 1024 + lane * 16);
#pragma unroll
      for (int ks = 0; ks < 4; ++ks)
#pragma unroll
        for (int T = 0; T < 4; ++T) o[T] = __builtin_amdgcn_mfma_f32_16x16x32_bf16(vf[ks * 4 + T], __builtin_bit_cast(bf16x8, pf[ks]), o[T], 0, 0, 0);
      const f32x4 f0 = __builtin_bit_cast(f32x4, l0), f1 = __builtin_bit_cast(f32x4, l1);
      const float lcu = ((f0.x + f0.y) + (f0.z + f0.w)) + ((f1.x + f1.y) + (f1.z + f1.w));
      const int row = ((16 * hgA + n) * 4 + (w >> 1)) * 128 + kb;
      asm volatile("" :: "v"(o[3]));
      STAMP(13);
      __builtin_amdgcn_sched_barrier(0);
      unsigned U = 0, V = 0;
#pragma unroll
      for (int T = 0; T < 4; T += 2) {
        unsigned a0 = pk2(o[T].x, o[T].y), a1 = pk2(o[T].z, o[T].w), b0 = pk2(o[T + 1].x, o[T + 1].y), b1 = pk2(o[T + 1].z, o[T + 1].w);
        auto s0 = __builtin_amdgcn_permlane16_swap(a0, b0, false, false); auto s1 = __builtin_amdgcn_permlane16_swap(a1, b1, false, false);
        const v4u ov = {s0[0], s1[0], s0[1], s1[1]};
        ST128(ov, WS_PO + row * 256 + (8 * (w & 1) + 2 * (T + (g & 1)) + (g >> 1)) * 16);
        cs(ov, U, V);
      }
      __builtin_amdgcn_sched_barrier(0);
      STAMP(14);
      U = x32addu(x16addu(U)); V = x32addu(x16addu(V));
      const unsigned mb = __float_as_uint(mcu), lb = __float_as_uint(lcu);
      if (g < 2) ST128((g == 0 ? v4u{mb, lb, d2(mb, 0x2B5D71C3u, d2(lb, 0x8F63E917u, U + elo)), d2(mb, 0xD1A34E8Bu, d2(lb, 0x47F92C6Du, V + ehi))} : v4u{elo, ehi, elo, ehi}), WS_PM + (row * 2 + (w & 1)) * 32 + g * 16); }
    cnt_add(L, F_PUB, lane);
    STAMP(4);
    v4u uv[2];
#pragma unroll
    for (int t = 0; t < 2; ++t) uv[t] = ldnt(a.wuv + ((size_t)(128 * cq + 16 * w + 8 * t + 2 * g + (n >> 3)) * 32 + h) * 128 + 64 * dh + 8 * (n & 7));
    __builtin_amdgcn_sched_barrier(0);
    { const int row0 = (h * 4 + cq) * 128 + 8 * w + g;
      v4u p[4], mt[4]; unsigned spins = 0;
      __builtin_amdgcn_s_sleep(SLH2);
      for (;;) {
        asm volatile("" ::: "memory");
#pragma unroll
        for (int t = 0; t < 4; ++t) { const int row = row0 + 4 * (t & 1) + 64 * (t >> 1); p[t] = LD128(WS_PO + row * 256 + n * 16); mt[t] = LD128(WS_PM + (row * 2 + (n >> 3)) * 32); }
        __builtin_amdgcn_sched_barrier(0);
        if (spins == 0) cnt_add(L, F_H2, lane);
        __builtin_amdgcn_sched_barrier(0);
        unsigned U = 0, V = 0, Um = 0, Vm = 0;
#pragma unroll
        for (int t = 0; t < 4; ++t) { cs(p[t], U, V);
          Um = d2(mt[t].x, 0x2B5D71C3u, d2(mt[t].y, 0x8F63E917u, Um - mt[t].z)); Vm = d2(mt[t].x, 0xD1A34E8Bu, d2(mt[t].y, 0x47F92C6Du, Vm - mt[t].w)); }
        if ((n & 7) == 0) { U += Um + 4u * elo; V += Vm + 4u * ehi; }
        if ((wsum(U) | wsum(V)) == 0u || ++spins > SPIN_CAP) break;
        const int to = WS_PM + ((((h * 4 + cq) * 128 + 64 * (lane >> 5) + 8 * w) * 2) * 32) + (lane & 31) * 16;
        for (;;) {
          __builtin_amdgcn_s_sleep(SLT);
          asm volatile("" ::: "memory");
          const v2u tg = LD64(to);
          if (__all((lane & 1) == 0 || (tg.x == elo && tg.y == ehi)) || ++spins > SPIN_CAP) break;
        }
      }
      STAMP(5);
      f32x4 acc = z4; float Mb[2], Lb[2];
#pragma unroll
      for (int b = 0; b < 2; ++b) {
        const float m0 = __uint_as_float(mt[2 * b].x), m1 = __uint_as_float(mt[2 * b + 1].x);
        Mb[b] = x32maxf(x16maxf(fmaxf(m0, m1)));
        const unsigned wp = pk2(__builtin_amdgcn_exp2f(m0 - Mb[b]), __builtin_amdgcn_exp2f(m1 - Mb[b]));
        const bool mine = (n >> 3) == b;
        acc = mfma(diag(dg, mine ? (wp & 0xffffu) : 0u), p[2 * b], acc); acc = mfma(diag(dg, mine ? (wp >> 16) : 0u), p[2 * b + 1], acc);
        Lb[b] = x32addf(x16addf(bflo(wp) * __uint_as_float(mt[2 * b].y) + bfhi(wp) * __uint_as_float(mt[2 * b + 1].y)));
      }
      *(volatile LAS f32x4*)(L + L_CP + ((w * 2 + (g >> 1)) * 128 + 8 * n + 4 * (g & 1)) * 4) = acc;
      if (lane < 2) *(volatile LAS v2u*)(L + L_CS + (lane * 8 + w) * 8) = v2u{min(__float_as_uint(lane ? Mb[1] : Mb[0]), SENT - 1u), min(__float_as_uint(lane ? Lb[1] : Lb[0]), SENT - 1u)}; }
    { const int b = n >> 3; float fk[8];
      { v4u s[4]; unsigned gv;
        for (;;) {
#pragma unroll
          for (int k = 0; k < 4; ++k) s[k] = lds_v4(L, L_CS + b * 64 + k * 16);
          gv = *(volatile LAS unsigned*)(L + L_G + b * 4);
          if (__all(nos(s[0]) & nos(s[1]) & nos(s[2]) & nos(s[3]) & (gv != SENT))) break;
          __builtin_amdgcn_s_sleep(SPN);
        }
        asm volatile("" ::: "memory");
        float M = -INFINITY, Ls = 0.f;
#pragma unroll
        for (int k = 0; k < 4; ++k) M = fmaxf(M, fmaxf(__uint_as_float(s[k].x), __uint_as_float(s[k].z)));
#pragma unroll
        for (int k = 0; k < 4; ++k) { fk[2 * k] = __builtin_amdgcn_exp2f(__uint_as_float(s[k].x) - M); fk[2 * k + 1] = __builtin_amdgcn_exp2f(__uint_as_float(s[k].z) - M);
          Ls = fmaf(fk[2 * k], __uint_as_float(s[k].y), fmaf(fk[2 * k + 1], __uint_as_float(s[k].w), Ls)); }
        const float sg = __uint_as_float(gv) * __builtin_amdgcn_rcpf(Ls);
        f32x2 c[16];
#pragma unroll
        for (int t = 0; t < 2; ++t)
#pragma unroll
          for (int k = 0; k < 8; ++k) c[t * 8 + k] = *(const LAS f32x2*)(L + L_CP + ((k * 2 + b) * 128 + 16 * w + 8 * t + 2 * g) * 4);
        f32x4 a0 = z4, a1 = z4;
#pragma unroll
        for (int t = 0; t < 2; ++t) { float s0 = 0.f, s1 = 0.f;
#pragma unroll
          for (int k = 0; k < 8; ++k) { s0 = fmaf(fk[k], c[t * 8 + k].x, s0); s1 = fmaf(fk[k], c[t * 8 + k].y, s1); }
          const unsigned sp = pk2(s0 * sg, s1 * sg);
          a0 = mfma(diag(dg, sp & 0xffffu), uv[t], a0); a1 = mfma(diag(dg, sp >> 16), uv[t], a1); }
        f32x4 v = n < 8 ? a0 : a1;
#pragma unroll
        for (int k = 0; k < 4; ++k) v[k] += dppf<ROR8>(v[k]);
        if (n < 8) *(volatile LAS f32x4*)(L + L_XP + ((w * 2 + (g >> 1)) * 64 + 8 * n + 4 * (g & 1)) * 4) = v; } }
    flag_set(L, F_XP + 4 * w, lane);
    if (w < 2) {
      flag_wait<8, SPN>(L, F_XP);
      float s = 0.f;
#pragma unroll
      for (int k = 0; k < 8; ++k) s += *(const LAS float*)(L + L_XP + ((k * 2 + w) * 64 + lane) * 4);
      *(volatile LAS unsigned*)(L + L_CTX + (w * 64 + (lane & 3) * 16 + (lane >> 2)) * 4) = min(__float_as_uint(s), SENT - 1u);
    }
    STAMP(6);
    if (w >= 2) {
      const int t6 = tid - 128, f = t6 >> 2, pt = t6 & 3, off = ((f / 48) * MD + 12 * (f % 48) + 4 * (pt < 3 ? pt : 0)) * 4;
      __builtin_amdgcn_s_setprio(0);
      flag_wait<7, 1>(L, F_OUT);
      __builtin_amdgcn_s_setprio(3);
      const f32x4 s = *(const LAS f32x4*)(L + L_OT + off) + *(const LAS f32x4*)(L + L_OF + off);
      v4u o = __builtin_bit_cast(v4u, s);
      unsigned U = 0, V = 0; cs(o, U, V); if (pt == 3) { U = elo; V = ehi; }
      U += dppu<XOR1>(U); U += dppu<XOR2>(U); V += dppu<XOR1>(V); V += dppu<XOR2>(V);
      if (pt == 3) o = v4u{U, V, 0u, 0u};
      ST128(o, WS_FO + f * 16384 + wg * 64 + pt * 16);
    }
    STAMP(7);
    if (wg < 96) {
      const int pt = lane & 3;
      v4u v0, v1; unsigned spins = 0;
      if (w < 2) flag_wait<7, 2>(L, F_OUT);
      __builtin_amdgcn_s_sleep(SLH3);
      for (;;) {
        asm volatile("" ::: "memory");
        v0 = LD128(WS_FO + wg * 16384 + tid * 16); v1 = LD128(WS_FO + wg * 16384 + 8192 + tid * 16);
        unsigned U = 0, V = 0; cs(v0, U, V); cs(v1, U, V);
        if (pt == 3) { U = 2u * elo - v0.x - v1.x; V = 2u * ehi - v0.y - v1.y; }
        if ((wsum(U) | wsum(V)) == 0u || ++spins > SPIN_CAP) break;
        __builtin_amdgcn_s_sleep(4);
      }
      f32x4 s = __builtin_bit_cast(f32x4, v0) + __builtin_bit_cast(f32x4, v1);
#pragma unroll
      for (int k = 0; k < 4; ++k) { float t = s[k]; t += dppf<ROR4>(t); t += dppf<ROR8>(t); t = x32addf(x16addf(t)); s[k] = t; }
      if (lane < 3) *(volatile LAS f32x4*)(L + L_FIN + w * 64 + lane * 16) = s;
      flag_set(L, F_FIN + 4 * w, lane);
      STAMP(11);
      if (w == 0) {
        const int b = wg / 48, col = 12 * (wg % 48) + (lane < 12 ? lane : 0);
        const float bb = (1.f - *(const LAS float*)(L + L_G + b * 4)) * bf1(a.b2[col]);
        flag_wait<8, SPN>(L, F_FIN);
        if (lane < 12) {
          float t = bb;
#pragma unroll
          for (int k = 0; k < 8; ++k) t += *(const LAS float*)(L + L_FIN + k * 64 + lane * 4);
          a.out[b * MD + col] = (u16)bfr(t);
          if (wg == 0 && lane == 0) __hip_atomic_store((GAS u64*)(a.ws + WS_EPOCH), ep, __ATOMIC_RELAXED, __HIP_MEMORY_SCOPE_AGENT);
        }
        STAMP(12);
        if (wg == 0) { asm volatile("s_waitcnt lgkmcnt(0)" ::: "memory");
          const unsigned t0 = *(volatile LAS unsigned*)(L + L_PR), tk = *(volatile LAS unsigned*)(L + L_PR + PROBE * 4), te = *(volatile LAS unsigned*)(L + L_PR + 12 * 4);
          const unsigned dur = 10u * (tk - t0);
          while ((unsigned)__builtin_amdgcn_s_memrealtime() - te < dur) __builtin_amdgcn_s_sleep(2); }
      }
    }
  } else if (wave < 10) {
    const int kw = wave - 8;
    __builtin_amdgcn_s_setprio(3);
    const u64 ep = __hip_atomic_load((const GAS u64*)(a.ws + WS_EPOCH), __ATOMIC_RELAXED, __HIP_MEMORY_SCOPE_AGENT) + 1ull;
    const unsigned elo = (unsigned)ep, ehi = (unsigned)(ep >> 32);
    { v4u uk[8];
#pragma unroll
      for (int rg = 0; rg < 8; ++rg) uk[rg] = ldnt(a.wuk + ((size_t)(128 * cq + 64 * kw + 8 * (n & 7) + rg) * 32 + h) * 128 + 64 * dh + 32 * (n >> 3) + 8 * g);
      __builtin_amdgcn_sched_barrier(0);
      cnt_add(L, F_ISS, lane);
      STAMP(8);
      flag_wait<8, SPN>(L, F_QP);
      STAMP(10);
      { f32x2 s = {0.f, 0.f};
#pragma unroll
        for (int k = 0; k < 8; ++k) s += *(const LAS f32x2*)(L + L_QP + ((k * 2 + (lane >> 5)) * 64 + 2 * (lane & 31)) * 4);
        *(volatile LAS unsigned*)(L + L_QB + kw * 256 + lane * 4) = pk2(s.x, s.y); }
      v4u qa0 = lds_v4(L, L_QB + kw * 256 + (n & 1) * 128 + g * 16), qa1 = lds_v4(L, L_QB + kw * 256 + (n & 1) * 128 + 64 + g * 16);
      if (n >= 2) { qa0 = v4u{0u, 0u, 0u, 0u}; qa1 = qa0; }
      constexpr float SC = 0.08838834764831845f * 1.4426950408889634f;
      float sel[8];
#pragma unroll
      for (int rg = 0; rg < 8; ++rg) {
        const f32x4 a1 = mfma(qa0, uk[rg], z4), a2 = mfma(qa1, uk[rg], z4);
        float v0 = n < 8 ? a1.x : a2.x, v1 = n < 8 ? a1.y : a2.y;
        v0 += dppf<ROR8>(v0); v1 += dppf<ROR8>(v1);
        sel[rg] = (n < 8 ? v0 : v1) * SC;
      }
      const v4u o = {pk2(sel[0], sel[1]), pk2(sel[2], sel[3]), pk2(sel[4], sel[5]), pk2(sel[6], sel[7])};
      const int qb = WS_QA + (((n >> 3) * 32 + h) * 8 + 2 * cq + kw) * 384;
      if (lane < 16) ST128(o, qb + dh * 128 + (n & 7) * 16);
      unsigned U = 0, V = 0; cs(o, U, V);
      U += dppu<XOR1>(U); U += dppu<XOR2>(U); U += dppu<HMIR>(U); V += dppu<XOR1>(V); V += dppu<XOR2>(V); V += dppu<HMIR>(V);
      if (lane < 16 && (n & 7) == 0) ST64((v2u{U + elo, V + ehi}), qb + 256 + dh * 8);
      cnt_add(L, F_SENT, lane); }
    __builtin_amdgcn_s_setprio(0);
    STAMP(1);
    cnt_wait<4>(L, F_PUB, 8u);
    f32x4 acc[3] = {z4, z4, z4};
#pragma unroll
    for (int pass = 0; pass < 2; ++pass) {
      const int it = wg + 256 * pass;
      if (pass == 1 && wg >= 32) break;
      const int hb = L_HID + pass * 128;
      v4u w2v[12];
      const u16* wp = a.w2 + (size_t)(16 * it + g) * MD + 384 * kw;
#pragma unroll
      for (int t = 0; t < 4; ++t) { w2v[t] = ldnt(wp + (size_t)4 * t * MD + 8 * n); w2v[4 + t] = ldnt(wp + (size_t)4 * t * MD + 128 + 8 * (kw ? (n & 7) : n)); }
      if (kw == 0) {
#pragma unroll
        for (int t = 0; t < 4; ++t) w2v[8 + t] = ldnt(wp + (size_t)4 * t * MD + 256 + 8 * n);
        const v2u b1p = *(const v2u*)(a.b1 + 16 * it + (n & 1) * 8 + 4 * (g & 1));
        v4u v[6]; unsigned spins = 0;
        for (;;) {
          asm volatile("" ::: "memory");
#pragma unroll
          for (int k = 0; k < 6; ++k) v[k] = LD128(WS_HX + ((it >> 3) * 6 + k) * 1152 + lane * 16);
          const v2u mt = LD64(WS_HX + ((it >> 3) * 6 + (lane < 6 ? lane : 0)) * 1152 + 1024);
          unsigned U = 0, V = 0;
#pragma unroll
          for (int k = 0; k < 6; ++k) cs(v[k], U, V);
          if (lane < 6) { U -= mt.x - elo; V -= mt.y - ehi; }
          if ((wsum(U) | wsum(V)) == 0u || ++spins > SPIN_CAP) break;
          __builtin_amdgcn_s_sleep(20);
        }
        unsigned gv; do { gv = *(volatile LAS unsigned*)(L + L_G + (g >> 1) * 4); } while (!__all(gv != SENT));
        f32x4 s = z4;
#pragma unroll
        for (int k = 0; k < 6; ++k) s += __builtin_bit_cast(f32x4, v[k]);
        if ((n >> 1) == (it & 7)) {
          const float og = 1.f - __uint_as_float(gv);
          f32x4 hv; hv.x = fmaxf(s.x + bflo(b1p.x), 0.f); hv.y = fmaxf(s.y + bfhi(b1p.x), 0.f); hv.z = fmaxf(s.z + bflo(b1p.y), 0.f); hv.w = fmaxf(s.w + bfhi(b1p.y), 0.f);
          *(volatile LAS f32x4*)(L + hb + ((g >> 1) * 16 + 8 * (n & 1) + 4 * (g & 1)) * 4) = hv * hv * og;
        }
        cnt_add(L, F_HID, lane);
      } else cnt_wait<4>(L, F_HID, (unsigned)pass + 1u);
#pragma unroll
      for (int t = 0; t < 4; ++t) { const v4u d = diag(dg, bfr(*(volatile LAS float*)(L + hb + ((n >> 3) * 16 + 4 * t + g) * 4)));
        acc[0] = mfma(d, w2v[t], acc[0]); acc[1] = mfma(d, w2v[4 + t], acc[1]); if (kw == 0) acc[2] = mfma(d, w2v[8 + t], acc[2]); }
    }
    { const int ob = L_OF + ((g >> 1) * MD + 384 * kw + 8 * n + 4 * (g & 1)) * 4;
      *(volatile LAS f32x4*)(L + ob) = acc[0];
      if (kw == 0 || n < 8) *(volatile LAS f32x4*)(L + ob + 512) = acc[1];
      if (kw == 0) *(volatile LAS f32x4*)(L + ob + 1024) = acc[2]; }
    flag_set(L, F_OUT + 4 * (5 + kw), lane);
    STAMP(3);
  } else if (wave < 15) {
    const int T = wave - 10;
    const u16* p = a.wo + (size_t)(h * 128 + 64 * dh) * MD;
    if (T < 4) {
      cnt_wait<1>(L, F_ISS, 10u);
      { const unsigned char* src = (const unsigned char*)(a.ckv + (size_t)kb * 128 * 512);
#pragma unroll 1
        for (int i = T; i < 128; i += 4) {
          const int row = (i + 64 * hgA) & 127;
          const int f = ((row & 3) << 2) | ((row >> 2) & 3);
          glds16_nt(src + row * 1024 + ((lane ^ f) * 16), (unsigned)(size_t)(L + L_KV) + row * 1024);
        } }
      cnt_add(L, F_DMAI, lane);
      STAMP(8);
      VMWAIT(0);
      flag_set(L, F_KV + 4 * T, lane);
      STAMP(1);
      cnt_wait<2>(L, F_GO, 8u);
      v4u wo[16];
#pragma unroll
      for (int t = 0; t < 16; ++t) {
        if (t == ST1) { __builtin_amdgcn_sched_barrier(0); cnt_wait<3>(L, F_PUB, 8u); }
        if (t == ST2) { __builtin_amdgcn_sched_barrier(0); cnt_wait<3>(L, F_H2, 8u); }
        wo[t] = ldnt(p + (size_t)(4 * ((t + 4 * cq) & 15) + g) * MD + 128 * T + 8 * n); }
      __builtin_amdgcn_sched_barrier(0);
      STAMP(2);
      while (*(volatile LAS unsigned*)(L + F_XP) == SENT) __builtin_amdgcn_s_sleep(4);
      __builtin_amdgcn_s_setprio(3);
      f32x4 acc = z4; v4u c[4];
      for (;;) {
#pragma unroll
        for (int t4 = 0; t4 < 4; ++t4) c[t4] = lds_v4(L, L_CTX + ((n >> 3) * 64 + g * 16 + 4 * ((t4 + cq) & 3)) * 4);
        if (__all(nos(c[0]) & nos(c[1]) & nos(c[2]) & nos(c[3]))) break;
        __builtin_amdgcn_s_sleep(SPN);
      }
#pragma unroll
      for (int t4 = 0; t4 < 4; ++t4) { const unsigned c01 = pk2(__uint_as_float(c[t4].x), __uint_as_float(c[t4].y)), c23 = pk2(__uint_as_float(c[t4].z), __uint_as_float(c[t4].w));
        acc = mfma(diag(dg, c01 & 0xffffu), wo[4 * t4], acc); acc = mfma(diag(dg, c01 >> 16), wo[4 * t4 + 1], acc);
        acc = mfma(diag(dg, c23 & 0xffffu), wo[4 * t4 + 2], acc); acc = mfma(diag(dg, c23 >> 16), wo[4 * t4 + 3], acc); }
      *(volatile LAS f32x4*)(L + L_OT + ((g >> 1) * MD + 128 * T + 8 * n + 4 * (g & 1)) * 4) = acc;
    } else {
      cnt_wait<4>(L, F_PUB, 8u);
      v4u wo[8];
#pragma unroll
      for (int t = 0; t < 8; ++t) wo[t] = ldnt(p + (size_t)(8 * t + 2 * g + (n >> 3)) * MD + 512 + 8 * (n & 7));
      __builtin_amdgcn_sched_barrier(0);
      while (*(volatile LAS unsigned*)(L + F_XP) == SENT) __builtin_amdgcn_s_sleep(4);
      __builtin_amdgcn_s_setprio(3);
      v4u c[8];
      for (;;) {
#pragma unroll
        for (int k = 0; k < 4; ++k) { c[k] = lds_v4(L, L_CTX + ((n >> 3) * 64 + (2 * (g & 1)) * 16 + 4 * k) * 4); c[4 + k] = lds_v4(L, L_CTX + ((n >> 3) * 64 + (2 * (g & 1) + 1) * 16 + 4 * k) * 4); }
        if (__all(nos(c[0]) & nos(c[1]) & nos(c[2]) & nos(c[3]) & nos(c[4]) & nos(c[5]) & nos(c[6]) & nos(c[7]))) break;
        __builtin_amdgcn_s_sleep(SPN);
      }
      f32x4 a0 = z4, a1 = z4;
#pragma unroll
      for (int t = 0; t < 8; ++t) { const int e = 2 * (t & 1);
        const unsigned u0 = (g >> 1) ? c[t >> 1][e + 1] : c[t >> 1][e], u1 = (g >> 1) ? c[4 + (t >> 1)][e + 1] : c[4 + (t >> 1)][e];
        const unsigned cp = pk2(__uint_as_float(u0), __uint_as_float(u1));
        a0 = mfma(diag(dg, cp & 0xffffu), wo[t], a0); a1 = mfma(diag(dg, cp >> 16), wo[t], a1); }
      f32x4 v = n < 8 ? a0 : a1;
#pragma unroll
      for (int k = 0; k < 4; ++k) v[k] += dppf<ROR8>(v[k]);
      if (n < 8) *(volatile LAS f32x4*)(L + L_OT + ((g >> 1) * MD + 512 + 8 * n + 4 * (g & 1)) * 4) = v;
    }
    flag_set(L, F_OUT + 4 * T, lane);
    STAMP(3);
  } else {
    asm volatile("s_waitcnt vmcnt(0)" ::: "memory");
    cnt_add(L, F_X, lane);
    const u64 ep = __hip_atomic_load((const GAS u64*)(a.ws + WS_EPOCH), __ATOMIC_RELAXED, __HIP_MEMORY_SCOPE_AGENT) + 1ull;
    const unsigned elo = (unsigned)ep, ehi = (unsigned)(ep >> 32);
    cnt_wait<1>(L, F_ISS, 10u);
    { float s0 = 0.f, s1 = 0.f;
      if (lane < 36) {
        const v4u gw = *(const v4u*)(a.gw + 8 * lane), gw2 = *(const v4u*)(a.gw + 288 + 8 * lane);
        const v4u xa = *(const LAS v4u*)(L + L_X + 16 * lane), xa2 = *(const LAS v4u*)(L + L_X + 576 + 16 * lane), xb = *(const LAS v4u*)(L + L_X + 1152 + 16 * lane), xb2 = *(const LAS v4u*)(L + L_X + 1728 + 16 * lane);
#pragma unroll
        for (int k = 0; k < 4; ++k) {
          s0 += bflo(gw[k]) * bflo(xa[k]) + bfhi(gw[k]) * bfhi(xa[k]) + bflo(gw2[k]) * bflo(xa2[k]) + bfhi(gw2[k]) * bfhi(xa2[k]);
          s1 += bflo(gw[k]) * bflo(xb[k]) + bfhi(gw[k]) * bfhi(xb[k]) + bflo(gw2[k]) * bflo(xb2[k]) + bfhi(gw2[k]) * bfhi(xb2[k]); }
      }
      s0 = wsumf(s0); s1 = wsumf(s1);
      if (lane < 2) *(volatile LAS unsigned*)(L + L_G + lane * 4) = min(__float_as_uint(1.f / (1.f + __expf(-(lane ? s1 : s0)))), SENT - 1u); }
    if (wg < 216) {
      const int T = wg / 6, rg = wg % 6;
      cnt_wait<4>(L, F_DMAI, 4u);
      const u16* p = a.w1 + (size_t)(96 * rg + 24 * g) * NF + 128 * T + 8 * n;
      v4u wv[24];
#pragma unroll
      for (int t = 0; t < 24; ++t) wv[t] = ldnt(p + (size_t)t * NF);
      __builtin_amdgcn_sched_barrier(0);
      v4u xs[3];
#pragma unroll
      for (int k = 0; k < 3; ++k) xs[k] = *(const LAS v4u*)(L + L_X + (n >> 3) * 1152 + (96 * rg + 24 * g) * 2 + 16 * k);
      f32x4 acc = z4;
#pragma unroll
      for (int t = 0; t < 24; ++t) { const unsigned xd = xs[t >> 3][(t >> 1) & 3]; acc = mfma(diag(dg, (t & 1) ? (xd >> 16) : (xd & 0xffffu)), wv[t], acc); }
      const v4u o = __builtin_bit_cast(v4u, acc);
      ST128(o, WS_HX + wg * 1152 + lane * 16);
      unsigned U = 0, V = 0; cs(o, U, V); U = wsum(U); V = wsum(V);
      if (lane == 0) ST64((v2u{U + elo, V + ehi}), WS_HX + wg * 1152 + 1024);
      STAMP(3);
    }
  }
  STAMP_OUT();
}
}

extern "C" void kernel_launch(void* const* d_in, const int* in_sizes, int n_in, void* d_out, int out_size, void* d_ws, size_t ws_size, hipStream_t stream) {
  Args a;
  a.x = (const u16*)d_in[0]; a.ckv = (const u16*)d_in[1]; a.wq = (const u16*)d_in[2]; a.wuk = (const u16*)d_in[3]; a.wuv = (const u16*)d_in[4]; a.wo = (const u16*)d_in[5];
  a.w1 = (const u16*)d_in[7]; a.b1 = (const u16*)d_in[8]; a.w2 = (const u16*)d_in[9]; a.b2 = (const u16*)d_in[10]; a.gw = (const u16*)d_in[11];
  a.out = (u16*)d_out; a.ws = (unsigned char*)d_ws;
  (void)hipFuncSetAttribute((const void*)fused_kernel, hipFuncAttributeMaxDynamicSharedMemorySize, L_END);
  hipLaunchKernelGGL(fused_kernel, dim3(256), dim3(1024), L_END, stream, a);
}
```

```cpp
#define PROBE 98
#include <hip/hip_runtime.h>
#include <hip/hip_bf16.h>
#include <stdint.h>

typedef unsigned short u16;
typedef unsigned long long u64;
typedef unsigned v4u __attribute__((ext_vector_type(4)));
typedef unsigned v2u __attribute__((ext_vector_type(2)));
typedef float f32x4 __attribute__((ext_vector_type(4)));
typedef float f32x2 __attribute__((ext_vector_type(2)));
typedef short bf16x8 __attribute__((ext_vector_type(8)));
typedef short s16x4 __attribute__((ext_vector_type(4)));
#define LAS __attribute__((address_space(3)))
#define GAS __attribute__((address_space(1)))
#define DI __device__ __forceinline__

namespace {
constexpr int MD = 576, NF = 4608;
constexpr int WS_EPOCH = 0;
constexpr int WS_QA = 4096;
constexpr int WS_HX = WS_QA + 512 * 384;
constexpr int WS_PM = WS_HX + 216 * 1152;
constexpr int WS_PO = WS_PM + 128 * 8192;
constexpr int WS_FO = WS_PO + 128 * 32768;
constexpr int WS_END = WS_FO + 96 * 16384;
constexpr int WS_STAMP = WS_END + 4096;
constexpr int L_KV = 0;
constexpr int L_QA = 131072;
constexpr int L_CP = L_QA, L_XP = L_QA + 8192;
constexpr int L_P = L_QA + 16384;
constexpr int L_QP = L_P;
constexpr int L_OT = L_KV;
constexpr int L_OF = L_P + 4096;
constexpr int L_X = L_OF + 4608;
constexpr int L_QB = L_X + 3072;
constexpr int L_HID = L_QB + 512;
constexpr int L_S0 = L_HID + 256;
constexpr int L_SM = L_S0;
constexpr int L_SS = L_SM + 512;
constexpr int L_CS = L_SS + 512;
constexpr int L_CTX = L_CS + 128;
constexpr int L_G = L_CTX + 512;
constexpr int L_FL = L_G + 16;
constexpr int F_QP = L_FL, F_QA = L_FL + 32, F_KV = L_FL + 64, F_XP = L_FL + 96, F_OUT = L_FL + 128, F_FIN = L_FL + 160, F_MISC = L_FL + 192;
constexpr int F_X = F_MISC, F_ISS = F_MISC + 4, F_SENT = F_MISC + 8, F_DMAI = F_MISC + 12, F_HID = F_MISC + 16, F_GO = F_MISC + 20, F_PUB = F_MISC + 24, F_H2 = F_MISC + 28;
constexpr int L_S1 = L_FL + 224;
constexpr int L_FIN = L_S1;
constexpr int L_PR = L_FIN + 512;
constexpr int L_END = L_PR + 256;
static_assert(L_END <= 163840 && (L_S1 - L_S0) % 16 == 0 && (L_S1 - L_S0) / 16 <= 128, "lds");
#define SPINCAP (1u << 18)
#define GOEARLY 1
#define SPN 1
#define SLT 6
#define ST1 6
#define ST2 11
#define SLH1 4
#define SLH2 24
#define SLH3 16
constexpr unsigned SPIN_CAP = SPINCAP, SENT = 0xFFFFFFFFu;
constexpr int AUX_SC1 = 16;

struct Args {
  const u16 *x, *ckv, *wq, *wuk, *wuv, *wo, *w1, *b1, *w2, *b2, *gw;
  u16* out;
  unsigned char* ws;
};

DI void bar() { asm volatile("s_waitcnt lgkmcnt(0)" ::: "memory"); __builtin_amdgcn_s_barrier(); asm volatile("" ::: "memory"); }
DI void lds_wait(LAS unsigned char* L, int off, unsigned want) {
  while (__hip_atomic_load((LAS unsigned*)(L + off), __ATOMIC_RELAXED, __HIP_MEMORY_SCOPE_WORKGROUP) < want) __builtin_amdgcn_s_sleep(1);
  asm volatile("" ::: "memory");
}
DI void lds_wait_fast(LAS unsigned char* L, int off, unsigned want) {
  while (__hip_atomic_load((LAS unsigned*)(L + off), __ATOMIC_RELAXED, __HIP_MEMORY_SCOPE_WORKGROUP) < want) { }
  asm volatile("" ::: "memory");
}
DI void lds_wait_slow(LAS unsigned char* L, int off, unsigned want) {
  while (__hip_atomic_load((LAS unsigned*)(L + off), __ATOMIC_RELAXED, __HIP_MEMORY_SCOPE_WORKGROUP) < want) __builtin_amdgcn_s_sleep(4);
  asm volatile("" ::: "memory");
}
DI void lds_set(LAS unsigned char* L, int off, int lane) {
  asm volatile("s_waitcnt lgkmcnt(0)" ::: "memory");
  if (lane == 0) (void)__hip_atomic_fetch_add((LAS unsigned*)(L + off), 1u, __ATOMIC_RELAXED, __HIP_MEMORY_SCOPE_WORKGROUP);
}
DI v4u ldnt(const void* p) { return *(const GAS v4u*)p; }
DI f32x4 mfma(v4u a, v4u b, f32x4 c) { return __builtin_amdgcn_mfma_f32_16x16x32_bf16(__builtin_bit_cast(bf16x8, a), __builtin_bit_cast(bf16x8, b), c, 0, 0, 0); }
DI float bflo(unsigned u) { return __uint_as_float(u << 16); }
DI float bfhi(unsigned u) { return __uint_as_float(u & 0xffff0000u); }
DI float bf1(u16 v) { return __uint_as_float(((unsigned)v) << 16); }
DI unsigned pk2(float lo, float hi) {
  typedef __bf16 bf2 __attribute__((ext_vector_type(2)));
  bf2 r; r.x = (__bf16)lo; r.y = (__bf16)hi; return __builtin_bit_cast(unsigned, r);
}
DI unsigned rotl(unsigned x, unsigned r) { return __builtin_amdgcn_alignbit(x, x, 32u - r); }
template <int CTRL> DI unsigned dppu(unsigned x) { return (unsigned)__builtin_amdgcn_mov_dpp((int)x, CTRL, 0xf, 0xf, true); }
template <int CTRL> DI float dppf(float x) { return __uint_as_float(dppu<CTRL>(__float_as_uint(x))); }
constexpr int XOR1 = 0xB1, XOR2 = 0x4E, HMIR = 0x141, ROR4 = 0x124, ROR8 = 0x128;
DI unsigned x16addu(unsigned x) { auto s = __builtin_amdgcn_permlane16_swap(x, x, false, false); return s[0] + s[1]; }
DI unsigned x32addu(unsigned x) { auto s = __builtin_amdgcn_permlane32_swap(x, x, false, false); return s[0] + s[1]; }
DI float x16addf(float x) { auto s = __builtin_amdgcn_permlane16_swap(__float_as_uint(x), __float_as_uint(x), false, false); return __uint_as_float(s[0]) + __uint_as_float(s[1]); }
DI float x32addf(float x) { auto s = __builtin_amdgcn_permlane32_swap(__float_as_uint(x), __float_as_uint(x), false, false); return __uint_as_float(s[0]) + __uint_as_float(s[1]); }
DI float x16maxf(float x) { auto s = __builtin_amdgcn_permlane16_swap(__float_as_uint(x), __float_as_uint(x), false, false); return fmaxf(__uint_as_float(s[0]), __uint_as_float(s[1])); }
DI float x32maxf(float x) { auto s = __builtin_amdgcn_permlane32_swap(__float_as_uint(x), __float_as_uint(x), false, false); return fmaxf(__uint_as_float(s[0]), __uint_as_float(s[1])); }
DI unsigned wsum(unsigned x) { x += dppu<XOR1>(x); x += dppu<XOR2>(x); x += dppu<ROR4>(x); x += dppu<ROR8>(x); return x32addu(x16addu(x)); }
DI float wsumf(float x) { x += dppf<XOR1>(x); x += dppf<XOR2>(x); x += dppf<ROR4>(x); x += dppf<ROR8>(x); return x32addf(x16addf(x)); }
DI unsigned d2(unsigned a, unsigned k, unsigned c) { typedef unsigned short us2 __attribute__((ext_vector_type(2)));
  return __builtin_amdgcn_udot2(__builtin_bit_cast(us2, a), __builtin_bit_cast(us2, k), c, false); }
DI void cs(v4u a, unsigned& U, unsigned& V) {
  U = d2(a.x, 0x9E37B5C5u, U); U = d2(a.y, 0x7A4DC2B3u, U); U = d2(a.z, 0xE4F1594Du, U); U = d2(a.w, 0x3B8FD16Fu, U);
  V = d2(a.x, 0x62E9F2A7u, V); V = d2(a.y, 0xC96B4F1Du, V); V = d2(a.z, 0x5573A9E5u, V); V = d2(a.w, 0xED2F8799u, V);
}
DI v4u lds_v4(LAS unsigned char* L, int off) { return *(volatile LAS v4u*)(L + off); }
DI bool nos(v4u v) { return (v.x != SENT) & (v.y != SENT) & (v.z != SENT) & (v.w != SENT); }
template <int N, int SLEEP> DI void flag_wait(LAS unsigned char* L, int off) {
  for (;;) { bool ok = nos(lds_v4(L, off)); if (N > 4) { const v4u b = lds_v4(L, off + 16); ok &= (b.x != SENT) & (N < 6 || b.y != SENT) & (N < 7 || b.z != SENT) & (N < 8 || b.w != SENT); }
    if (ok) break; if (SLEEP) __builtin_amdgcn_s_sleep(SLEEP); }
  asm volatile("" ::: "memory");
}
DI void flag_set(LAS unsigned char* L, int off, int lane) { asm volatile("" ::: "memory"); if (lane == 0) *(volatile LAS unsigned*)(L + off) = 1u; asm volatile("" ::: "memory"); }
template <int SLEEP> DI void cnt_wait(LAS unsigned char* L, int off, unsigned n) {
  while (*(volatile LAS unsigned*)(L + off) + 1u < n) { if (SLEEP) __builtin_amdgcn_s_sleep(SLEEP); }
  asm volatile("" ::: "memory");
}
DI void cnt_add(LAS unsigned char* L, int off, int lane) { asm volatile("" ::: "memory"); if (lane == 0) (void)__hip_atomic_fetch_add((LAS unsigned*)(L + off), 1u, __ATOMIC_RELAXED, __HIP_MEMORY_SCOPE_WORKGROUP); asm volatile("" ::: "memory"); }
struct Diag { unsigned m0, m1, m2, m3, sh; };
DI Diag make_diag(int i) { const int p = i & 7; Diag d; d.sh = (p & 1) * 16; d.m0 = (p >> 1) == 0 ? ~0u : 0u; d.m1 = (p >> 1) == 1 ? ~0u : 0u; d.m2 = (p >> 1) == 2 ? ~0u : 0u; d.m3 = (p >> 1) == 3 ? ~0u : 0u; return d; }
DI v4u diag(const Diag& d, unsigned vb16) { const unsigned v = vb16 << d.sh; return v4u{v & d.m0, v & d.m1, v & d.m2, v & d.m3}; }
DI unsigned bfr(float f) { return pk2(f, 0.f) & 0xffffu; }
DI void glds16_nt(const void* gsrc, unsigned lds_dst) {
  unsigned keep;
  asm volatile("s_mov_b32 %0, m0\n\ts_mov_b32 m0, %2\n\ts_nop 0\n\tglobal_load_lds_dwordx4 %1, off\n\ts_mov_b32 m0, %0" : "=&s"(keep) : "v"(gsrc), "s"(lds_dst) : "memory");
}
#define LD128(off) __builtin_amdgcn_raw_buffer_load_b128(rs, (off), 0, AUX_SC1)
#define LD64(off) __builtin_amdgcn_raw_buffer_load_b64(rs, (off), 0, AUX_SC1)
#define ST128(v, off) __builtin_amdgcn_raw_buffer_store_b128((v), rs, (off), 0, AUX_SC1)
#define ST64(v, off) __builtin_amdgcn_raw_buffer_store_b64((v), rs, (off), 0, AUX_SC1)
#define VMWAIT(N) do { __builtin_amdgcn_sched_barrier(0); asm volatile("s_waitcnt vmcnt(%0)" :: "n"(N) : "memory"); __builtin_amdgcn_sched_barrier(0); } while (0)
#define STAMP(k) do { if (wg == 0 && (wave == 0 || wave == 8 || wave == 10) && lane == 0) *(volatile LAS unsigned*)(L + L_PR + ((wave == 0 ? 0 : wave == 8 ? 16 : 32) + (k)) * 4) = (unsigned)__builtin_amdgcn_s_memrealtime(); } while (0)
#define STAMP_OUT() do { } while (0)

__global__ void __launch_bounds__(1024) fused_kernel(Args a) {
  u64 tfirst; asm volatile("s_memrealtime %0" : "=s"(tfirst));
  extern __shared__ __attribute__((aligned(16))) unsigned char smem[];
  LAS unsigned char* L = (LAS unsigned char*)smem;
  const int tid = threadIdx.x, lane = tid & 63, wave = __builtin_amdgcn_readfirstlane(tid >> 6);
  const int wg = blockIdx.x, xl = wg & 7, r = wg >> 3;
  const int n = lane & 15, g = lane >> 4;
  const int h = xl + 8 * (r >> 3), j = r & 7, cq = j >> 1, dh = j & 1;
  const int hgA = r >> 4, kb = xl * 16 + (r & 15), cb = kb >> 6;
  const __amdgpu_buffer_rsrc_t rs = __builtin_amdgcn_make_buffer_rsrc(a.ws, 0, WS_END, 0x00020000);
  const Diag dg = make_diag(n);
  const f32x4 z4 = {0.f, 0.f, 0.f, 0.f};
  STAMP(0);
  if (wave == 15) {
    const unsigned char* xs = (const unsigned char*)a.x;
    glds16_nt(xs + lane * 16, (unsigned)(size_t)(L + L_X)); glds16_nt(xs + 1024 + lane * 16, (unsigned)(size_t)(L + L_X) + 1024);
    glds16_nt(xs + 2048 + (lane < 16 ? lane : 15) * 16, (unsigned)(size_t)(L + L_X) + 2048); }
  if (tid < (L_S1 - L_S0) / 16) *(LAS v4u*)(L + L_S0 + tid * 16) = v4u{SENT, SENT, SENT, SENT};
  bar();

  if (wave < 8) {
    const int w = wave;
    __builtin_amdgcn_s_setprio(3);
    { v4u wv[9];
      const u16* p = a.wq + (size_t)(2 * g + (n >> 3)) * 4096 + h * 128 + dh * 64 + 8 * (n & 7);
#pragma unroll
      for (int k = 0; k < 9; ++k) { int I = w + 8 * k + 18 * cq; I = I >= 72 ? I - 72 : I; wv[k] = ldnt(p + (size_t)I * (8 * 4096)); }
      __builtin_amdgcn_sched_barrier(0);
      cnt_add(L, F_ISS, lane);
      STAMP(8);
      cnt_wait<1>(L, F_X, 1u);
      unsigned xv[9];
#pragma unroll
      for (int k = 0; k < 9; ++k) { int I = w + 8 * k + 18 * cq; I = I >= 72 ? I - 72 : I; xv[k] = *(const LAS unsigned*)(L + L_X + (n >> 3) * 1152 + (8 * I + 2 * g) * 2); }
      f32x4 a0 = z4, a1 = z4;
#pragma unroll
      for (int k = 0; k < 9; ++k) { a0 = mfma(diag(dg, xv[k] & 0xffffu), wv[k], a0); a1 = mfma(diag(dg, xv[k] >> 16), wv[k], a1); }
      f32x4 v = n < 8 ? a0 : a1;
#pragma unroll
      for (int k = 0; k < 4; ++k) v[k] += dppf<ROR8>(v[k]);
      if (n < 8) *(LAS f32x4*)(L + L_QP + ((w * 2 + (g >> 1)) * 64 + 8 * n + 4 * (g & 1)) * 4) = v;
      flag_set(L, F_QP + 4 * w, lane); }
    STAMP(1);
    const u64 ep = __hip_atomic_load((const GAS u64*)(a.ws + WS_EPOCH), __ATOMIC_RELAXED, __HIP_MEMORY_SCOPE_AGENT) + 1ull;
    const unsigned elo = (unsigned)ep, ehi = (unsigned)(ep >> 32);
    __builtin_amdgcn_s_setprio(0);
    cnt_wait<2>(L, F_SENT, 2u);
    flag_wait<4, 2>(L, F_KV);
    __builtin_amdgcn_s_setprio(3);
    v4u kf[16];
    { const int k8 = lane & 7, hq = lane >> 3;
      const int qb = WS_QA + ((cb * 32 + 16 * hgA + hq) * 8 + w) * 384 + k8 * 16, mo = WS_QA + ((cb * 32 + 16 * hgA + n) * 8 + w) * 384 + 256;
      v4u v0, v1, v2, v3; unsigned spins = 0;
      for (;;) {
        asm volatile("" ::: "memory");
        v0 = LD128(qb); v1 = LD128(qb + 128); v2 = LD128(qb + 8 * 3072); v3 = LD128(qb + 8 * 3072 + 128);
        const v4u mt = LD128(mo);
        __builtin_amdgcn_sched_barrier(0);
        if (spins == 0) {
          cnt_add(L, F_GO, lane);
          const int f = ((n & 3) << 2) | (n >> 2);
          const int rowb = L_KV + (16 * w + n) * 1024;
#pragma unroll
          for (int s = 0; s < 16; ++s) kf[s] = *(const LAS v4u*)(L + rowb + (s >> 2) * 256 + (((4 * g + (s & 3)) ^ f) * 16)); }
        __builtin_amdgcn_sched_barrier(0);
        unsigned U = 0, V = 0; cs(v0, U, V); cs(v1, U, V); cs(v2, U, V); cs(v3, U, V);
        if (lane < 16) { U -= mt.x + mt.z - 2u * elo; V -= mt.y + mt.w - 2u * ehi; }
        if ((wsum(U) | wsum(V)) == 0u || ++spins > SPIN_CAP) break;
        __builtin_amdgcn_s_sleep(6);
      }
      auto add2 = [](v4u p, v4u q) { v4u o;
        o.x = pk2(bflo(p.x) + bflo(q.x), bfhi(p.x) + bfhi(q.x)); o.y = pk2(bflo(p.y) + bflo(q.y), bfhi(p.y) + bfhi(q.y));
        o.z = pk2(bflo(p.z) + bflo(q.z), bfhi(p.z) + bfhi(q.z)); o.w = pk2(bflo(p.w) + bflo(q.w), bfhi(p.w) + bfhi(q.w)); return o; };
      const int wb = L_QA + (4 * (w >> 1) + (k8 & 3)) * 1024 + (2 * (w & 1) + (k8 >> 2)) * 256, rot = hq + 4 * (k8 & 3) + 2 * (w & 1);
      *(LAS v4u*)(L + wb + (rot & 15) * 16) = add2(v0, v1);
      *(LAS v4u*)(L + wb + ((rot + 8) & 15) * 16) = add2(v2, v3);
      flag_set(L, F_QA + 4 * w, lane);
    }
    STAMP(2);
    flag_wait<8, SPN>(L, F_QA);
    STAMP(3);
    f32x4 sc = z4, sc2 = z4;
#pragma unroll
    for (int hs = 0; hs < 2; ++hs) {
      v4u qf[8];
#pragma unroll
      for (int s = 0; s < 8; ++s) qf[s] = *(const LAS v4u*)(L + L_QA + (8 * hs + s) * 1024 + g * 256 + ((n + 4 * (s & 3) + 2 * (g >> 1)) & 15) * 16);
      __builtin_amdgcn_sched_barrier(0);
#pragma unroll
      for (int s = 0; s < 8; s += 2) { sc = mfma(kf[8 * hs + s], qf[s], sc); sc2 = mfma(kf[8 * hs + s + 1], qf[s + 1], sc2); }
    }
    sc += sc2;
    { float m = fmaxf(fmaxf(sc.x, sc.y), fmaxf(sc.z, sc.w)); m = x32maxf(x16maxf(m));
      if (g == 0) *(volatile LAS unsigned*)(L + L_SM + n * 32 + w * 4) = min(__float_as_uint(m), SENT - 1u); }
    float mcu;
    { v4u m0, m1;
      for (;;) { m0 = lds_v4(L, L_SM + n * 32); m1 = lds_v4(L, L_SM + n * 32 + 16); if (__all(nos(m0) & nos(m1))) break; __builtin_amdgcn_s_sleep(SPN); }
      STAMP(9);
      const f32x4 f0 = __builtin_bit_cast(f32x4, m0), f1 = __builtin_bit_cast(f32x4, m1);
      mcu = fmaxf(fmaxf(fmaxf(f0.x, f0.y), fmaxf(f0.z, f0.w)), fmaxf(fmaxf(f1.x, f1.y), fmaxf(f1.z, f1.w)));
      const unsigned p01 = pk2(__builtin_amdgcn_exp2f(sc.x - mcu), __builtin_amdgcn_exp2f(sc.y - mcu));
      const unsigned p23 = pk2(__builtin_amdgcn_exp2f(sc.z - mcu), __builtin_amdgcn_exp2f(sc.w - mcu));
      *(volatile LAS v2u*)(L + L_P + (w >> 1) * 1024 + (n + 16 * (2 * (w & 1) + (g >> 1))) * 16 + 8 * (g & 1)) = v2u{p01, p23};
      float l = (bflo(p01) + bfhi(p01)) + (bflo(p23) + bfhi(p23)); l = x32addf(x16addf(l));
      if (g == 0) *(volatile LAS unsigned*)(L + L_SS + n * 32 + w * 4) = min(__float_as_uint(l), SENT - 1u); }
    { const int q4 = n >> 2, p4 = n & 3;
      const unsigned base = L_KV + (8 * g + q4) * 1024 + (w >> 1) * 256 + ((((8 * (w & 1)) + (p4 >> 1)) ^ ((q4 << 2) | (2 * (g & 1)))) * 16) + 8 * (p4 & 1);
      bf16x8 vf[16];
#pragma unroll
      for (int ks = 0; ks < 4; ++ks)
#pragma unroll
        for (int T = 0; T < 4; ++T) {
          const s16x4 lo = __builtin_amdgcn_ds_read_tr16_b64_v4i16((LAS s16x4*)(L + ((base ^ (unsigned)(T << 5)) + ks * 32768)));
          const s16x4 hi = __builtin_amdgcn_ds_read_tr16_b64_v4i16((LAS s16x4*)(L + ((base ^ (unsigned)((T << 5) | (1 << 4) | (1 << 12))) + ks * 32768)));
          vf[ks * 4 + T] = __builtin_shufflevector(lo, hi, 0, 1, 2, 3, 4, 5, 6, 7); }
      __builtin_amdgcn_sched_barrier(0);
      v4u l0, l1;
      for (;;) { l0 = lds_v4(L, L_SS + n * 32); l1 = lds_v4(L, L_SS + n * 32 + 16); if (__all(nos(l0) & nos(l1))) break; __builtin_amdgcn_s_sleep(SPN); }
      asm volatile("" ::: "memory");
      STAMP(10);
      f32x4 o[4] = {z4, z4, z4, z4};
      v4u pf[4];
#pragma unroll
      for (int ks = 0; ks < 4; ++ks) pf[ks] = *(const LAS v4u*)(L + L_P + ks * 1024 + lane * 16);
#pragma unroll
      for (int ks = 0; ks < 4; ++ks)
#pragma unroll
        for (int T = 0; T < 4; ++T) o[T] = __builtin_amdgcn_mfma_f32_16x16x32_bf16(vf[ks * 4 + T], __builtin_bit_cast(bf16x8, pf[ks]), o[T], 0, 0, 0);
      const f32x4 f0 = __builtin_bit_cast(f32x4, l0), f1 = __builtin_bit_cast(f32x4, l1);
      const float lcu = ((f0.x + f0.y) + (f0.z + f0.w)) + ((f1.x + f1.y) + (f1.z + f1.w));
      const int row = ((16 * hgA + n) * 4 + (w >> 1)) * 128 + kb;
      asm volatile("" :: "v"(o[3]));
      STAMP(13);
      __builtin_amdgcn_sched_barrier(0);
      unsigned U = 0, V = 0;
#pragma unroll
      for (int T = 0; T < 4; T += 2) {
        unsigned a0 = pk2(o[T].x, o[T].y), a1 = pk2(o[T].z, o[T].w), b0 = pk2(o[T + 1].x, o[T + 1].y), b1 = pk2(o[T + 1].z, o[T + 1].w);
        auto s0 = __builtin_amdgcn_permlane16_swap(a0, b0, false, false); auto s1 = __builtin_amdgcn_permlane16_swap(a1, b1, false, false);
        const v4u ov = {s0[0], s1[0], s0[1], s1[1]};
        ST128(ov, WS_PO + row * 256 + (8 * (w & 1) + 2 * (T + (g & 1)) + (g >> 1)) * 16);
        cs(ov, U, V);
      }
      __builtin_amdgcn_sched_barrier(0);
      STAMP(14);
      U = x32addu(x16addu(U)); V = x32addu(x16addu(V));
      const unsigned mb = __float_as_uint(mcu), lb = __float_as_uint(lcu);
      if (g < 2) ST128((g == 0 ? v4u{mb, lb, d2(mb, 0x2B5D71C3u, d2(lb, 0x8F63E917u, U + elo)), d2(mb, 0xD1A34E8Bu, d2(lb, 0x47F92C6Du, V + ehi))} : v4u{elo, ehi, elo, ehi}), WS_PM + (row * 2 + (w & 1)) * 32 + g * 16); }
    cnt_add(L, F_PUB, lane);
    STAMP(4);
    v4u uv[2];
#pragma unroll
    for (int t = 0; t < 2; ++t) uv[t] = ldnt(a.wuv + ((size_t)(128 * cq + 16 * w + 8 * t + 2 * g + (n >> 3)) * 32 + h) * 128 + 64 * dh + 8 * (n & 7));
    __builtin_amdgcn_sched_barrier(0);
    { const int row0 = (h * 4 + cq) * 128 + 8 * w + g;
      v4u p[4], mt[4]; unsigned spins = 0;
      __builtin_amdgcn_s_sleep(SLH2);
      for (;;) {
        asm volatile("" ::: "memory");
#pragma unroll
        for (int t = 0; t < 4; ++t) { const int row = row0 + 4 * (t & 1) + 64 * (t >> 1); p[t] = LD128(WS_PO + row * 256 + n * 16); mt[t] = LD128(WS_PM + (row * 2 + (n >> 3)) * 32); }
        __builtin_amdgcn_sched_barrier(0);
        if (spins == 0) cnt_add(L, F_H2, lane);
        __builtin_amdgcn_sched_barrier(0);
        unsigned U = 0, V = 0, Um = 0, Vm = 0;
#pragma unroll
        for (int t = 0; t < 4; ++t) { cs(p[t], U, V);
          Um = d2(mt[t].x, 0x2B5D71C3u, d2(mt[t].y, 0x8F63E917u, Um - mt[t].z)); Vm = d2(mt[t].x, 0xD1A34E8Bu, d2(mt[t].y, 0x47F92C6Du, Vm - mt[t].w)); }
        if ((n & 7) == 0) { U += Um + 4u * elo; V += Vm + 4u * ehi; }
        if ((wsum(U) | wsum(V)) == 0u || ++spins > SPIN_CAP) break;
        const int to = WS_PM + ((((h * 4 + cq) * 128 + 64 * (lane >> 5) + 8 * w) * 2) * 32) + (lane & 31) * 16;
        for (;;) {
          __builtin_amdgcn_s_sleep(SLT);
          asm volatile("" ::: "memory");
          const v2u tg = LD64(to);
          if (__all((lane & 1) == 0 || (tg.x == elo && tg.y == ehi)) || ++spins > SPIN_CAP) break;
        }
      }
      STAMP(5);
      f32x4 acc = z4; float Mb[2], Lb[2];
#pragma unroll
      for (int b = 0; b < 2; ++b) {
        const float m0 = __uint_as_float(mt[2 * b].x), m1 = __uint_as_float(mt[2 * b + 1].x);
        Mb[b] = x32maxf(x16maxf(fmaxf(m0, m1)));
        const unsigned wp = pk2(__builtin_amdgcn_exp2f(m0 - Mb[b]), __builtin_amdgcn_exp2f(m1 - Mb[b]));
        const bool mine = (n >> 3) == b;
        acc = mfma(diag(dg, mine ? (wp & 0xffffu) : 0u), p[2 * b], acc); acc = mfma(diag(dg, mine ? (wp >> 16) : 0u), p[2 * b + 1], acc);
        Lb[b] = x32addf(x16addf(bflo(wp) * __uint_as_float(mt[2 * b].y) + bfhi(wp) * __uint_as_float(mt[2 * b + 1].y)));
      }
      *(volatile LAS f32x4*)(L + L_CP + ((w * 2 + (g >> 1)) * 128 + 8 * n + 4 * (g & 1)) * 4) = acc;
      if (lane < 2) *(volatile LAS v2u*)(L + L_CS + (lane * 8 + w) * 8) = v2u{min(__float_as_uint(lane ? Mb[1] : Mb[0]), SENT - 1u), min(__float_as_uint(lane ? Lb[1] : Lb[0]), SENT - 1u)}; }
    { const int b = n >> 3; float fk[8];
      { v4u s[4]; unsigned gv;
        for (;;) {
#pragma unroll
          for (int k = 0; k < 4; ++k) s[k] = lds_v4(L, L_CS + b * 64 + k * 16);
          gv = *(volatile LAS unsigned*)(L + L_G + b * 4);
          if (__all(nos(s[0]) & nos(s[1]) & nos(s[2]) & nos(s[3]) & (gv != SENT))) break;
          __builtin_amdgcn_s_sleep(SPN);
        }
        asm volatile("" ::: "memory");
        float M = -INFINITY, Ls = 0.f;
#pragma unroll
        for (int k = 0; k < 4; ++k) M = fmaxf(M, fmaxf(__uint_as_float(s[k].x), __uint_as_float(s[k].z)));
#pragma unroll
        for (int k = 0; k < 4; ++k) { fk[2 * k] = __builtin_amdgcn_exp2f(__uint_as_float(s[k].x) - M); fk[2 * k + 1] = __builtin_amdgcn_exp2f(__uint_as_float(s[k].z) - M);
          Ls = fmaf(fk[2 * k], __uint_as_float(s[k].y), fmaf(fk[2 * k + 1], __uint_as_float(s[k].w), Ls)); }
        const float sg = __uint_as_float(gv) * __builtin_amdgcn_rcpf(Ls);
        f32x2 c[16];
#pragma unroll
        for (int t = 0; t < 2; ++t)
#pragma unroll
          for (int k = 0; k < 8; ++k) c[t * 8 + k] = *(const LAS f32x2*)(L + L_CP + ((k * 2 + b) * 128 + 16 * w + 8 * t + 2 * g) * 4);
        f32x4 a0 = z4, a1 = z4;
#pragma unroll
        for (int t = 0; t < 2; ++t) { float s0 = 0.f, s1 = 0.f;
#pragma unroll
          for (int k = 0; k < 8; ++k) { s0 = fmaf(fk[k], c[t * 8 + k].x, s0); s1 = fmaf(fk[k], c[t * 8 + k].y, s1); }
          const unsigned sp = pk2(s0 * sg, s1 * sg);
          a0 = mfma(diag(dg, sp & 0xffffu), uv[t], a0); a1 = mfma(diag(dg, sp >> 16), uv[t], a1); }
        f32x4 v = n < 8 ? a0 : a1;
#pragma unroll
        for (int k = 0; k < 4; ++k) v[k] += dppf<ROR8>(v[k]);
        if (n < 8) *(volatile LAS f32x4*)(L + L_XP + ((w * 2 + (g >> 1)) * 64 + 8 * n + 4 * (g & 1)) * 4) = v; } }
    flag_set(L, F_XP + 4 * w, lane);
    if (w < 2) {
      flag_wait<8, SPN>(L, F_XP);
      float s = 0.f;
#pragma unroll
      for (int k = 0; k < 8; ++k) s += *(const LAS float*)(L + L_XP + ((k * 2 + w) * 64 + lane) * 4);
      *(volatile LAS unsigned*)(L + L_CTX + (w * 64 + (lane & 3) * 16 + (lane >> 2)) * 4) = min(__float_as_uint(s), SENT - 1u);
    }
    STAMP(6);
    if (w >= 2) {
      const int t6 = tid - 128, f = t6 >> 2, pt = t6 & 3, off = ((f / 48) * MD + 12 * (f % 48) + 4 * (pt < 3 ? pt : 0)) * 4;
      __builtin_amdgcn_s_setprio(0);
      flag_wait<7, 1>(L, F_OUT);
      __builtin_amdgcn_s_setprio(3);
      const f32x4 s = *(const LAS f32x4*)(L + L_OT + off) + *(const LAS f32x4*)(L + L_OF + off);
      v4u o = __builtin_bit_cast(v4u, s);
      unsigned U = 0, V = 0; cs(o, U, V); if (pt == 3) { U = elo; V = ehi; }
      U += dppu<XOR1>(U); U += dppu<XOR2>(U); V += dppu<XOR1>(V); V += dppu<XOR2>(V);
      if (pt == 3) o = v4u{U, V, 0u, 0u};
      ST128(o, WS_FO + f * 16384 + wg * 64 + pt * 16);
    }
    STAMP(7);
    if (wg < 96) {
      const int pt = lane & 3;
      v4u v0, v1; unsigned spins = 0;
      if (w < 2) flag_wait<7, 2>(L, F_OUT);
      __builtin_amdgcn_s_sleep(SLH3);
      for (;;) {
        asm volatile("" ::: "memory");
        v0 = LD128(WS_FO + wg * 16384 + tid * 16); v1 = LD128(WS_FO + wg * 16384 + 8192 + tid * 16);
        unsigned U = 0, V = 0; cs(v0, U, V); cs(v1, U, V);
        if (pt == 3) { U = 2u * elo - v0.x - v1.x; V = 2u * ehi - v0.y - v1.y; }
        if ((wsum(U) | wsum(V)) == 0u || ++spins > SPIN_CAP) break;
        __builtin_amdgcn_s_sleep(4);
      }
      f32x4 s = __builtin_bit_cast(f32x4, v0) + __builtin_bit_cast(f32x4, v1);
#pragma unroll
      for (int k = 0; k < 4; ++k) { float t = s[k]; t += dppf<ROR4>(t); t += dppf<ROR8>(t); t = x32addf(x16addf(t)); s[k] = t; }
      if (lane < 3) *(volatile LAS f32x4*)(L + L_FIN + w * 64 + lane * 16) = s;
      flag_set(L, F_FIN + 4 * w, lane);
      STAMP(11);
      if (w == 0) {
        const int b = wg / 48, col = 12 * (wg % 48) + (lane < 12 ? lane : 0);
        const float bb = (1.f - *(const LAS float*)(L + L_G + b * 4)) * bf1(a.b2[col]);
        flag_wait<8, SPN>(L, F_FIN);
        if (lane < 12) {
          float t = bb;
#pragma unroll
          for (int k = 0; k < 8; ++k) t += *(const LAS float*)(L + L_FIN + k * 64 + lane * 4);
          a.out[b * MD + col] = (u16)bfr(t);
          if (wg == 0 && lane == 0) __hip_atomic_store((GAS u64*)(a.ws + WS_EPOCH), ep, __ATOMIC_RELAXED, __HIP_MEMORY_SCOPE_AGENT);
        }
        STAMP(12);
        if (wg == 0) { asm volatile("s_waitcnt lgkmcnt(0)" ::: "memory");
          const unsigned t0 = *(volatile LAS unsigned*)(L + L_PR), tk = *(volatile LAS unsigned*)(L + L_PR + (PROBE % 64) * 4), te = *(volatile LAS unsigned*)(L + L_PR + 12 * 4);
          const unsigned dur = 10u * ((unsigned)tfirst - *(const unsigned*)(a.ws + 2048));
          while ((unsigned)__builtin_amdgcn_s_memrealtime() - te < dur) __builtin_amdgcn_s_sleep(2); }
      }
    }
  } else if (wave < 10) {
    const int kw = wave - 8;
    __builtin_amdgcn_s_setprio(3);
    const u64 ep = __hip_atomic_load((const GAS u64*)(a.ws + WS_EPOCH), __ATOMIC_RELAXED, __HIP_MEMORY_SCOPE_AGENT) + 1ull;
    const unsigned elo = (unsigned)ep, ehi = (unsigned)(ep >> 32);
    { v4u uk[8];
#pragma unroll
      for (int rg = 0; rg < 8; ++rg) uk[rg] = ldnt(a.wuk + ((size_t)(128 * cq + 64 * kw + 8 * (n & 7) + rg) * 32 + h) * 128 + 64 * dh + 32 * (n >> 3) + 8 * g);
      __builtin_amdgcn_sched_barrier(0);
      cnt_add(L, F_ISS, lane);
      STAMP(8);
      flag_wait<8, SPN>(L, F_QP);
      STAMP(10);
      { f32x2 s = {0.f, 0.f};
#pragma unroll
        for (int k = 0; k < 8; ++k) s += *(const LAS f32x2*)(L + L_QP + ((k * 2 + (lane >> 5)) * 64 + 2 * (lane & 31)) * 4);
        *(volatile LAS unsigned*)(L + L_QB + kw * 256 + lane * 4) = pk2(s.x, s.y); }
      v4u qa0 = lds_v4(L, L_QB + kw * 256 + (n & 1) * 128 + g * 16), qa1 = lds_v4(L, L_QB + kw * 256 + (n & 1) * 128 + 64 + g * 16);
      if (n >= 2) { qa0 = v4u{0u, 0u, 0u, 0u}; qa1 = qa0; }
      constexpr float SC = 0.08838834764831845f * 1.4426950408889634f;
      float sel[8];
#pragma unroll
      for (int rg = 0; rg < 8; ++rg) {
        const f32x4 a1 = mfma(qa0, uk[rg], z4), a2 = mfma(qa1, uk[rg], z4);
        float v0 = n < 8 ? a1.x : a2.x, v1 = n < 8 ? a1.y : a2.y;
        v0 += dppf<ROR8>(v0); v1 += dppf<ROR8>(v1);
        sel[rg] = (n < 8 ? v0 : v1) * SC;
      }
      const v4u o = {pk2(sel[0], sel[1]), pk2(sel[2], sel[3]), pk2(sel[4], sel[5]), pk2(sel[6], sel[7])};
      const int qb = WS_QA + (((n >> 3) * 32 + h) * 8 + 2 * cq + kw) * 384;
      if (lane < 16) ST128(o, qb + dh * 128 + (n & 7) * 16);
      unsigned U = 0, V = 0; cs(o, U, V);
      U += dppu<XOR1>(U); U += dppu<XOR2>(U); U += dppu<HMIR>(U); V += dppu<XOR1>(V); V += dppu<XOR2>(V); V += dppu<HMIR>(V);
      if (lane < 16 && (n & 7) == 0) ST64((v2u{U + elo, V + ehi}), qb + 256 + dh * 8);
      cnt_add(L, F_SENT, lane); }
    __builtin_amdgcn_s_setprio(0);
    STAMP(1);
    cnt_wait<4>(L, F_PUB, 8u);
    f32x4 acc[3] = {z4, z4, z4};
#pragma unroll
    for (int pass = 0; pass < 2; ++pass) {
      const int it = wg + 256 * pass;
      if (pass == 1 && wg >= 32) break;
      const int hb = L_HID + pass * 128;
      v4u w2v[12];
      const u16* wp = a.w2 + (size_t)(16 * it + g) * MD + 384 * kw;
#pragma unroll
      for (int t = 0; t < 4; ++t) { w2v[t] = ldnt(wp + (size_t)4 * t * MD + 8 * n); w2v[4 + t] = ldnt(wp + (size_t)4 * t * MD + 128 + 8 * (kw ? (n & 7) : n)); }
      if (kw == 0) {
#pragma unroll
        for (int t = 0; t < 4; ++t) w2v[8 + t] = ldnt(wp + (size_t)4 * t * MD + 256 + 8 * n);
        const v2u b1p = *(const v2u*)(a.b1 + 16 * it + (n & 1) * 8 + 4 * (g & 1));
        v4u v[6]; unsigned spins = 0;
        for (;;) {
          asm volatile("" ::: "memory");
#pragma unroll
          for (int k = 0; k < 6; ++k) v[k] = LD128(WS_HX + ((it >> 3) * 6 + k) * 1152 + lane * 16);
          const v2u mt = LD64(WS_HX + ((it >> 3) * 6 + (lane < 6 ? lane : 0)) * 1152 + 1024);
          unsigned U = 0, V = 0;
#pragma unroll
          for (int k = 0; k < 6; ++k) cs(v[k], U, V);
          if (lane < 6) { U -= mt.x - elo; V -= mt.y - ehi; }
          if ((wsum(U) | wsum(V)) == 0u || ++spins > SPIN_CAP) break;
          __builtin_amdgcn_s_sleep(20);
        }
        unsigned gv; do { gv = *(volatile LAS unsigned*)(L + L_G + (g >> 1) * 4); } while (!__all(gv != SENT));
        f32x4 s = z4;
#pragma unroll
        for (int k = 0; k < 6; ++k) s += __builtin_bit_cast(f32x4, v[k]);
        if ((n >> 1) == (it & 7)) {
          const float og = 1.f - __uint_as_float(gv);
          f32x4 hv; hv.x = fmaxf(s.x + bflo(b1p.x), 0.f); hv.y = fmaxf(s.y + bfhi(b1p.x), 0.f); hv.z = fmaxf(s.z + bflo(b1p.y), 0.f); hv.w = fmaxf(s.w + bfhi(b1p.y), 0.f);
          *(volatile LAS f32x4*)(L + hb + ((g >> 1) * 16 + 8 * (n & 1) + 4 * (g & 1)) * 4) = hv * hv * og;
        }
        cnt_add(L, F_HID, lane);
      } else cnt_wait<4>(L, F_HID, (unsigned)pass + 1u);
#pragma unroll
      for (int t = 0; t < 4; ++t) { const v4u d = diag(dg, bfr(*(volatile LAS float*)(L + hb + ((n >> 3) * 16 + 4 * t + g) * 4)));
        acc[0] = mfma(d, w2v[t], acc[0]); acc[1] = mfma(d, w2v[4 + t], acc[1]); if (kw == 0) acc[2] = mfma(d, w2v[8 + t], acc[2]); }
    }
    { const int ob = L_OF + ((g >> 1) * MD + 384 * kw + 8 * n + 4 * (g & 1)) * 4;
      *(volatile LAS f32x4*)(L + ob) = acc[0];
      if (kw == 0 || n < 8) *(volatile LAS f32x4*)(L + ob + 512) = acc[1];
      if (kw == 0) *(volatile LAS f32x4*)(L + ob + 1024) = acc[2]; }
    flag_set(L, F_OUT + 4 * (5 + kw), lane);
    STAMP(3);
  } else if (wave < 15) {
    const int T = wave - 10;
    const u16* p = a.wo + (size_t)(h * 128 + 64 * dh) * MD;
    if (T < 4) {
      cnt_wait<1>(L, F_ISS, 10u);
      { const unsigned char* src = (const unsigned char*)(a.ckv + (size_t)kb * 128 * 512);
#pragma unroll 1
        for (int i = T; i < 128; i += 4) {
          const int row = (i + 64 * hgA) & 127;
          const int f = ((row & 3) << 2) | ((row >> 2) & 3);
          glds16_nt(src + row * 1024 + ((lane ^ f) * 16), (unsigned)(size_t)(L + L_KV) + row * 1024);
        } }
      cnt_add(L, F_DMAI, lane);
      STAMP(8);
      VMWAIT(0);
      flag_set(L, F_KV + 4 * T, lane);
      STAMP(1);
      cnt_wait<2>(L, F_GO, 8u);
      v4u wo[16];
#pragma unroll
      for (int t = 0; t < 16; ++t) {
        if (t == ST1) { __builtin_amdgcn_sched_barrier(0); cnt_wait<3>(L, F_PUB, 8u); }
        if (t == ST2) { __builtin_amdgcn_sched_barrier(0); cnt_wait<3>(L, F_H2, 8u); }
        wo[t] = ldnt(p + (size_t)(4 * ((t + 4 * cq) & 15) + g) * MD + 128 * T + 8 * n); }
      __builtin_amdgcn_sched_barrier(0);
      STAMP(2);
      while (*(volatile LAS unsigned*)(L + F_XP) == SENT) __builtin_amdgcn_s_sleep(4);
      __builtin_amdgcn_s_setprio(3);
      f32x4 acc = z4; v4u c[4];
      for (;;) {
#pragma unroll
        for (int t4 = 0; t4 < 4; ++t4) c[t4] = lds_v4(L, L_CTX + ((n >> 3) * 64 + g * 16 + 4 * ((t4 + cq) & 3)) * 4);
        if (__all(nos(c[0]) & nos(c[1]) & nos(c[2]) & nos(c[3]))) break;
        __builtin_amdgcn_s_sleep(SPN);
      }
#pragma unroll
      for (int t4 = 0; t4 < 4; ++t4) { const unsigned c01 = pk2(__uint_as_float(c[t4].x), __uint_as_float(c[t4].y)), c23 = pk2(__uint_as_float(c[t4].z), __uint_as_float(c[t4].w));
        acc = mfma(diag(dg, c01 & 0xffffu), wo[4 * t4], acc); acc = mfma(diag(dg, c01 >> 16), wo[4 * t4 + 1], acc);
        acc = mfma(diag(dg, c23 & 0xffffu), wo[4 * t4 + 2], acc); acc = mfma(diag(dg, c23 >> 16), wo[4 * t4 + 3], acc); }
      *(volatile LAS f32x4*)(L + L_OT + ((g >> 1) * MD + 128 * T + 8 * n + 4 * (g & 1)) * 4) = acc;
    } else {
      cnt_wait<4>(L, F_PUB, 8u);
      v4u wo[8];
#pragma unroll
      for (int t = 0; t < 8; ++t) wo[t] = ldnt(p + (size_t)(8 * t + 2 * g + (n >> 3)) * MD + 512 + 8 * (n & 7));
      __builtin_amdgcn_sched_barrier(0);
      while (*(volatile LAS unsigned*)(L + F_XP) == SENT) __builtin_amdgcn_s_sleep(4);
      __builtin_amdgcn_s_setprio(3);
      v4u c[8];
      for (;;) {
#pragma unroll
        for (int k = 0; k < 4; ++k) { c[k] = lds_v4(L, L_CTX + ((n >> 3) * 64 + (2 * (g & 1)) * 16 + 4 * k) * 4); c[4 + k] = lds_v4(L, L_CTX + ((n >> 3) * 64 + (2 * (g & 1) + 1) * 16 + 4 * k) * 4); }
        if (__all(nos(c[0]) & nos(c[1]) & nos(c[2]) & nos(c[3]) & nos(c[4]) & nos(c[5]) & nos(c[6]) & nos(c[7]))) break;
        __builtin_amdgcn_s_sleep(SPN);
      }
      f32x4 a0 = z4, a1 = z4;
#pragma unroll
      for (int t = 0; t < 8; ++t) { const int e = 2 * (t & 1);
        const unsigned u0 = (g >> 1) ? c[t >> 1][e + 1] : c[t >> 1][e], u1 = (g >> 1) ? c[4 + (t >> 1)][e + 1] : c[4 + (t >> 1)][e];
        const unsigned cp = pk2(__uint_as_float(u0), __uint_as_float(u1));
        a0 = mfma(diag(dg, cp & 0xffffu), wo[t], a0); a1 = mfma(diag(dg, cp >> 16), wo[t], a1); }
      f32x4 v = n < 8 ? a0 : a1;
#pragma unroll
      for (int k = 0; k < 4; ++k) v[k] += dppf<ROR8>(v[k]);
      if (n < 8) *(volatile LAS f32x4*)(L + L_OT + ((g >> 1) * MD + 512 + 8 * n + 4 * (g & 1)) * 4) = v;
    }
    flag_set(L, F_OUT + 4 * T, lane);
    STAMP(3);
  } else {
    asm volatile("s_waitcnt vmcnt(0)" ::: "memory");
    cnt_add(L, F_X, lane);
    const u64 ep = __hip_atomic_load((const GAS u64*)(a.ws + WS_EPOCH), __ATOMIC_RELAXED, __HIP_MEMORY_SCOPE_AGENT) + 1ull;
    const unsigned elo = (unsigned)ep, ehi = (unsigned)(ep >> 32);
    cnt_wait<1>(L, F_ISS, 10u);
    { float s0 = 0.f, s1 = 0.f;
      if (lane < 36) {
        const v4u gw = *(const v4u*)(a.gw + 8 * lane), gw2 = *(const v4u*)(a.gw + 288 + 8 * lane);
        const v4u xa = *(const LAS v4u*)(L + L_X + 16 * lane), xa2 = *(const LAS v4u*)(L + L_X + 576 + 16 * lane), xb = *(const LAS v4u*)(L + L_X + 1152 + 16 * lane), xb2 = *(const LAS v4u*)(L + L_X + 1728 + 16 * lane);
#pragma unroll
        for (int k = 0; k < 4; ++k) {
          s0 += bflo(gw[k]) * bflo(xa[k]) + bfhi(gw[k]) * bfhi(xa[k]) + bflo(gw2[k]) * bflo(xa2[k]) + bfhi(gw2[k]) * bfhi(xa2[k]);
          s1 += bflo(gw[k]) * bflo(xb[k]) + bfhi(gw[k]) * bfhi(xb[k]) + bflo(gw2[k]) * bflo(xb2[k]) + bfhi(gw2[k]) * bfhi(xb2[k]); }
      }
      s0 = wsumf(s0); s1 = wsumf(s1);
      if (lane < 2) *(volatile LAS unsigned*)(L + L_G + lane * 4) = min(__float_as_uint(1.f / (1.f + __expf(-(lane ? s1 : s0)))), SENT - 1u); }
    if (wg < 216) {
      const int T = wg / 6, rg = wg % 6;
      cnt_wait<4>(L, F_DMAI, 4u);
      const u16* p = a.w1 + (size_t)(96 * rg + 24 * g) * NF + 128 * T + 8 * n;
      v4u wv[24];
#pragma unroll
      for (int t = 0; t < 24; ++t) wv[t] = ldnt(p + (size_t)t * NF);
      __builtin_amdgcn_sched_barrier(0);
      v4u xs[3];
#pragma unroll
      for (int k = 0; k < 3; ++k) xs[k] = *(const LAS v4u*)(L + L_X + (n >> 3) * 1152 + (96 * rg + 24 * g) * 2 + 16 * k);
      f32x4 acc = z4;
#pragma unroll
      for (int t = 0; t < 24; ++t) { const unsigned xd = xs[t >> 3][(t >> 1) & 3]; acc = mfma(diag(dg, (t & 1) ? (xd >> 16) : (xd & 0xffffu)), wv[t], acc); }
      const v4u o = __builtin_bit_cast(v4u, acc);
      ST128(o, WS_HX + wg * 1152 + lane * 16);
      unsigned U = 0, V = 0; cs(o, U, V); U = wsum(U); V = wsum(V);
      if (lane == 0) ST64((v2u{U + elo, V + ehi}), WS_HX + wg * 1152 + 1024);
      STAMP(3);
    }
  }
  STAMP_OUT();
}
__global__ void pre_kernel(unsigned char* ws) { if (threadIdx.x == 0) *(u64*)(ws + 2048) = __builtin_amdgcn_s_memrealtime(); }
}

extern "C" void kernel_launch(void* const* d_in, const int* in_sizes, int n_in, void* d_out, int out_size, void* d_ws, size_t ws_size, hipStream_t stream) {
  Args a;
  a.x = (const u16*)d_in[0]; a.ckv = (const u16*)d_in[1]; a.wq = (const u16*)d_in[2]; a.wuk = (const u16*)d_in[3]; a.wuv = (const u16*)d_in[4]; a.wo = (const u16*)d_in[5];
  a.w1 = (const u16*)d_in[7]; a.b1 = (const u16*)d_in[8]; a.w2 = (const u16*)d_in[9]; a.b2 = (const u16*)d_in[10]; a.gw = (const u16*)d_in[11];
  a.out = (u16*)d_out; a.ws = (unsigned char*)d_ws;
  (void)hipFuncSetAttribute((const void*)fused_kernel, hipFuncAttributeMaxDynamicSharedMemorySize, L_END);
  hipLaunchKernelGGL(pre_kernel, dim3(1), dim3(64), 0, stream, (unsigned char*)d_ws);
  hipLaunchKernelGGL(fused_kernel, dim3(256), dim3(1024), L_END, stream, a);
}
```

```cpp
#include <hip/hip_runtime.h>
#include <hip/hip_bf16.h>
#include <stdint.h>

typedef unsigned short u16;
typedef unsigned long long u64;
typedef unsigned v4u __attribute__((ext_vector_type(4)));
typedef unsigned v2u __attribute__((ext_vector_type(2)));
typedef float f32x4 __attribute__((ext_vector_type(4)));
typedef float f32x2 __attribute__((ext_vector_type(2)));
typedef short bf16x8 __attribute__((ext_vector_type(8)));
typedef short s16x4 __attribute__((ext_vector_type(4)));
#define LAS __attribute__((address_space(3)))
#define GAS __attribute__((address_space(1)))
#define DI __device__ __forceinline__

namespace {
constexpr int MD = 576, NF = 4608;
constexpr int WS_EPOCH = 0;
constexpr int WS_QA = 4096;
constexpr int WS_HX = WS_QA + 512 * 384;
constexpr int WS_PM = WS_HX + 216 * 1152;
constexpr int WS_PO = WS_PM + 128 * 8192;
constexpr int WS_FO = WS_PO + 128 * 32768;
constexpr int WS_END = WS_FO + 96 * 16384;
constexpr int WS_STAMP = WS_END + 4096;
constexpr int L_KV = 0;
constexpr int L_QA = 131072;
constexpr int L_CP = L_QA, L_XP = L_QA + 8192;
constexpr int L_P = L_QA + 16384;
constexpr int L_QP = L_P;
constexpr int L_OT = L_KV;
constexpr int L_OF = L_P + 4096;
constexpr int L_X = L_OF + 4608;
constexpr int L_QB = L_X + 3072;
constexpr int L_HID = L_QB + 512;
constexpr int L_S0 = L_HID + 256;
constexpr int L_SM = L_S0;
constexpr int L_SS = L_SM + 512;
constexpr int L_CS = L_SS + 512;
constexpr int L_CTX = L_CS + 128;
constexpr int L_G = L_CTX + 512;
constexpr int L_FL = L_G + 16;
constexpr int F_QP = L_FL, F_QA = L_FL + 8, F_KV = L_FL + 16, F_XP = L_FL + 24, F_OUT = L_FL + 32, F_FIN = L_FL + 40, F_SM = L_FL + 48, F_SS = L_FL + 56, F_CS = L_FL + 64, F_CTX = L_FL + 72, F_MISC = L_FL + 192;
constexpr int F_X = F_MISC, F_ISS = F_MISC + 4, F_SENT = F_MISC + 8, F_DMAI = F_MISC + 12, F_HID = F_MISC + 16, F_GO = F_MISC + 20, F_PUB = F_MISC + 24, F_H2 = F_MISC + 28;
constexpr int L_S1 = L_FL + 224;
constexpr int L_FIN = L_S1;
constexpr int L_END = L_FIN + 512;
static_assert(L_END <= 163840 && (L_S1 - L_S0) % 16 == 0 && (L_S1 - L_S0) / 16 <= 128, "lds");
#define SPINCAP (1u << 18)
#define GOEARLY 1
#define SPN 0
#define SLT 3
#define ST1 12
#define ST2 16
#define CODEPF 28000
#define WKV 5
#define NTH 8
#define SLH1 4
#define SLH2 36
#define SLH3 16
constexpr unsigned SPIN_CAP = SPINCAP, SENT = 0xFFFFFFFFu;
constexpr int AUX_SC1 = 16;

struct Args {
  const u16 *x, *ckv, *wq, *wuk, *wuv, *wo, *w1, *b1, *w2, *b2, *gw;
  u16* out;
  unsigned char* ws;
};

DI void bar() { asm volatile("s_waitcnt lgkmcnt(0)" ::: "memory"); __builtin_amdgcn_s_barrier(); asm volatile("" ::: "memory"); }
DI void lds_wait(LAS unsigned char* L, int off, unsigned want) {
  while (__hip_atomic_load((LAS unsigned*)(L + off), __ATOMIC_RELAXED, __HIP_MEMORY_SCOPE_WORKGROUP) < want) __builtin_amdgcn_s_sleep(1);
  asm volatile("" ::: "memory");
}
DI void lds_wait_fast(LAS unsigned char* L, int off, unsigned want) {
  while (__hip_atomic_load((LAS unsigned*)(L + off), __ATOMIC_RELAXED, __HIP_MEMORY_SCOPE_WORKGROUP) < want) { }
  asm volatile("" ::: "memory");
}
DI void lds_wait_slow(LAS unsigned char* L, int off, unsigned want) {
  while (__hip_atomic_load((LAS unsigned*)(L + off), __ATOMIC_RELAXED, __HIP_MEMORY_SCOPE_WORKGROUP) < want) __builtin_amdgcn_s_sleep(4);
  asm volatile("" ::: "memory");
}
DI void lds_set(LAS unsigned char* L, int off, int lane) {
  asm volatile("s_waitcnt lgkmcnt(0)" ::: "memory");
  if (lane == 0) (void)__hip_atomic_fetch_add((LAS unsigned*)(L + off), 1u, __ATOMIC_RELAXED, __HIP_MEMORY_SCOPE_WORKGROUP);
}
DI v4u ldnt(const void* p) { return *(const GAS v4u*)p; }
DI f32x4 mfma(v4u a, v4u b, f32x4 c) { return __builtin_amdgcn_mfma_f32_16x16x32_bf16(__builtin_bit_cast(bf16x8, a), __builtin_bit_cast(bf16x8, b), c, 0, 0, 0); }
DI float bflo(unsigned u) { return __uint_as_float(u << 16); }
DI float bfhi(unsigned u) { return __uint_as_float(u & 0xffff0000u); }
DI float bf1(u16 v) { return __uint_as_float(((unsigned)v) << 16); }
DI unsigned pk2(float lo, float hi) {
  typedef __bf16 bf2 __attribute__((ext_vector_type(2)));
  bf2 r; r.x = (__bf16)lo; r.y = (__bf16)hi; return __builtin_bit_cast(unsigned, r);
}
DI unsigned rotl(unsigned x, unsigned r) { return __builtin_amdgcn_alignbit(x, x, 32u - r); }
template <int CTRL> DI unsigned dppu(unsigned x) { return (unsigned)__builtin_amdgcn_mov_dpp((int)x, CTRL, 0xf, 0xf, true); }
template <int CTRL> DI float dppf(float x) { return __uint_as_float(dppu<CTRL>(__float_as_uint(x))); }
constexpr int XOR1 = 0xB1, XOR2 = 0x4E, HMIR = 0x141, ROR4 = 0x124, ROR8 = 0x128;
DI unsigned x16addu(unsigned x) { auto s = __builtin_amdgcn_permlane16_swap(x, x, false, false); return s[0] + s[1]; }
DI unsigned x32addu(unsigned x) { auto s = __builtin_amdgcn_permlane32_swap(x, x, false, false); return s[0] + s[1]; }
DI float x16addf(float x) { auto s = __builtin_amdgcn_permlane16_swap(__float_as_uint(x), __float_as_uint(x), false, false); return __uint_as_float(s[0]) + __uint_as_float(s[1]); }
DI float x32addf(float x) { auto s = __builtin_amdgcn_permlane32_swap(__float_as_uint(x), __float_as_uint(x), false, false); return __uint_as_float(s[0]) + __uint_as_float(s[1]); }
DI float x16maxf(float x) { auto s = __builtin_amdgcn_permlane16_swap(__float_as_uint(x), __float_as_uint(x), false, false); return fmaxf(__uint_as_float(s[0]), __uint_as_float(s[1])); }
DI float x32maxf(float x) { auto s = __builtin_amdgcn_permlane32_swap(__float_as_uint(x), __float_as_uint(x), false, false); return fmaxf(__uint_as_float(s[0]), __uint_as_float(s[1])); }
DI unsigned wsum(unsigned x) { x += dppu<XOR1>(x); x += dppu<XOR2>(x); x += dppu<ROR4>(x); x += dppu<ROR8>(x); return x32addu(x16addu(x)); }
DI float wsumf(float x) { x += dppf<XOR1>(x); x += dppf<XOR2>(x); x += dppf<ROR4>(x); x += dppf<ROR8>(x); return x32addf(x16addf(x)); }
DI unsigned d2(unsigned a, unsigned k, unsigned c) { typedef unsigned short us2 __attribute__((ext_vector_type(2)));
  return __builtin_amdgcn_udot2(__builtin_bit_cast(us2, a), __builtin_bit_cast(us2, k), c, false); }
DI void cs(v4u a, unsigned& U, unsigned& V) {
  U = d2(a.x, 0x9E37B5C5u, U); U = d2(a.y, 0x7A4DC2B3u, U); U = d2(a.z, 0xE4F1594Du, U); U = d2(a.w, 0x3B8FD16Fu, U);
  V = d2(a.x, 0x62E9F2A7u, V); V = d2(a.y, 0xC96B4F1Du, V); V = d2(a.z, 0x5573A9E5u, V); V = d2(a.w, 0xED2F8799u, V);
}
DI v4u lds_v4(LAS unsigned char* L, int off) { return *(volatile LAS v4u*)(L + off); }
DI void lds_rd2(unsigned ad, v4u& a, v4u& b) { asm volatile("ds_read_b128 %0, %2\n\tds_read_b128 %1, %2 offset:16\n\ts_waitcnt lgkmcnt(0)" : "=&v"(a), "=&v"(b) : "v"(ad) : "memory"); }
DI void lds_rd4(unsigned ad, v4u& a, v4u& b, v4u& c, v4u& d) {
  asm volatile("ds_read_b128 %0, %4\n\tds_read_b128 %1, %4 offset:16\n\tds_read_b128 %2, %4 offset:32\n\tds_read_b128 %3, %4 offset:48\n\ts_waitcnt lgkmcnt(0)" : "=&v"(a), "=&v"(b), "=&v"(c), "=&v"(d) : "v"(ad) : "memory"); }
DI void lds_rd8(unsigned ad, v4u (&c)[8]) {
  asm volatile("ds_read_b128 %0, %8\n\tds_read_b128 %1, %8 offset:16\n\tds_read_b128 %2, %8 offset:32\n\tds_read_b128 %3, %8 offset:48\n\t"
               "ds_read_b128 %4, %8 offset:64\n\tds_read_b128 %5, %8 offset:80\n\tds_read_b128 %6, %8 offset:96\n\tds_read_b128 %7, %8 offset:112\n\ts_waitcnt lgkmcnt(0)"
               : "=&v"(c[0]), "=&v"(c[1]), "=&v"(c[2]), "=&v"(c[3]), "=&v"(c[4]), "=&v"(c[5]), "=&v"(c[6]), "=&v"(c[7]) : "v"(ad) : "memory"); }
DI void lds_rd16x2(unsigned ad, f32x2 (&c)[16]) {
  asm volatile("ds_read_b64 %0, %16\n\tds_read_b64 %1, %16 offset:1024\n\tds_read_b64 %2, %16 offset:2048\n\tds_read_b64 %3, %16 offset:3072\n\t"
               "ds_read_b64 %4, %16 offset:4096\n\tds_read_b64 %5, %16 offset:5120\n\tds_read_b64 %6, %16 offset:6144\n\tds_read_b64 %7, %16 offset:7168\n\t"
               "ds_read_b64 %8, %16 offset:32\n\tds_read_b64 %9, %16 offset:1056\n\tds_read_b64 %10, %16 offset:2080\n\tds_read_b64 %11, %16 offset:3104\n\t"
               "ds_read_b64 %12, %16 offset:4128\n\tds_read_b64 %13, %16 offset:5152\n\tds_read_b64 %14, %16 offset:6176\n\tds_read_b64 %15, %16 offset:7200\n\ts_waitcnt lgkmcnt(0)"
               : "=&v"(c[0]), "=&v"(c[1]), "=&v"(c[2]), "=&v"(c[3]), "=&v"(c[4]), "=&v"(c[5]), "=&v"(c[6]), "=&v"(c[7]),
                 "=&v"(c[8]), "=&v"(c[9]), "=&v"(c[10]), "=&v"(c[11]), "=&v"(c[12]), "=&v"(c[13]), "=&v"(c[14]), "=&v"(c[15]) : "v"(ad) : "memory"); }
DI bool nos(v4u v) { return (v.x != SENT) & (v.y != SENT) & (v.z != SENT) & (v.w != SENT); }
template <int N, int SLEEP> DI void flag_wait(LAS unsigned char* L, int off) {
  constexpr u64 MASK = N == 8 ? ~0ull : ((1ull << (8 * (N & 7))) - 1ull);
  while ((*(volatile LAS u64*)(L + off) & MASK) != 0ull) { if (SLEEP) __builtin_amdgcn_s_sleep(SLEEP); }
  asm volatile("" ::: "memory");
}
DI void flag_set(LAS unsigned char* L, int off, int lane) { asm volatile("" ::: "memory"); if (lane == 0) *(volatile LAS unsigned char*)(L + off) = 0; asm volatile("" ::: "memory"); }
template <int SLEEP> DI void cnt_wait(LAS unsigned char* L, int off, unsigned n) {
  while (*(volatile LAS unsigned*)(L + off) + 1u < n) { if (SLEEP) __builtin_amdgcn_s_sleep(SLEEP); }
  asm volatile("" ::: "memory");
}
DI void cnt_add(LAS unsigned char* L, int off, int lane) { asm volatile("" ::: "memory"); if (lane == 0) (void)__hip_atomic_fetch_add((LAS unsigned*)(L + off), 1u, __ATOMIC_RELAXED, __HIP_MEMORY_SCOPE_WORKGROUP); asm volatile("" ::: "memory"); }
struct Diag { unsigned m0, m1, m2, m3, sh; };
DI Diag make_diag(int i) { const int p = i & 7; Diag d; d.sh = (p & 1) * 16; d.m0 = (p >> 1) == 0 ? ~0u : 0u; d.m1 = (p >> 1) == 1 ? ~0u : 0u; d.m2 = (p >> 1) == 2 ? ~0u : 0u; d.m3 = (p >> 1) == 3 ? ~0u : 0u; return d; }
DI v4u diag(const Diag& d, unsigned vb16) { const unsigned v = vb16 << d.sh; return v4u{v & d.m0, v & d.m1, v & d.m2, v & d.m3}; }
DI unsigned bfr(float f) { return pk2(f, 0.f) & 0xffffu; }
DI void glds16_nt(const void* gsrc, unsigned lds_dst) {
  unsigned keep;
  asm volatile("s_mov_b32 %0, m0\n\ts_mov_b32 m0, %2\n\ts_nop 0\n\tglobal_load_lds_dwordx4 %1, off\n\ts_mov_b32 m0, %0" : "=&s"(keep) : "v"(gsrc), "s"(lds_dst) : "memory");
}
#define LD128(off) __builtin_amdgcn_raw_buffer_load_b128(rs, (off), 0, AUX_SC1)
#define LD64(off) __builtin_amdgcn_raw_buffer_load_b64(rs, (off), 0, AUX_SC1)
#define ST128(v, off) __builtin_amdgcn_raw_buffer_store_b128((v), rs, (off), 0, AUX_SC1)
#define ST64(v, off) __builtin_amdgcn_raw_buffer_store_b64((v), rs, (off), 0, AUX_SC1)
#define VMWAIT(N) do { __builtin_amdgcn_sched_barrier(0); asm volatile("s_waitcnt vmcnt(%0)" :: "n"(N) : "memory"); __builtin_amdgcn_sched_barrier(0); } while (0)
#define STAMP(k) do { } while (0)
#define STAMP_OUT() do { } while (0)

__global__ void __launch_bounds__(1024) fused_kernel(Args a) {
  u64 pc0; asm volatile("s_getpc_b64 %0" : "=s"(pc0));
  extern __shared__ __attribute__((aligned(16))) unsigned char smem[];
  LAS unsigned char* L = (LAS unsigned char*)smem;
  const int tid = threadIdx.x, lane = tid & 63, wave = __builtin_amdgcn_readfirstlane(tid >> 6);
  const int wg = blockIdx.x, xl = wg & 7, r = wg >> 3;
  const int n = lane & 15, g = lane >> 4;
  const int h = xl + 8 * (r >> 3), j = r & 7, cq = j >> 1, dh = j & 1;
  const int hgA = r >> 4, kb = xl * 16 + (r & 15), cb = kb >> 6;
  const __amdgpu_buffer_rsrc_t rs = __builtin_amdgcn_make_buffer_rsrc(a.ws, 0, WS_END, 0x00020000);
  const Diag dg = make_diag(n);
  const f32x4 z4 = {0.f, 0.f, 0.f, 0.f};
  STAMP(0);
  if (__builtin_expect(wave < 8, 1)) {
    const int w = wave;
    if (w == 0) { const unsigned char* xs = (const unsigned char*)a.x;
      glds16_nt(xs + lane * 16, (unsigned)(size_t)(L + L_X)); glds16_nt(xs + 1024 + lane * 16, (unsigned)(size_t)(L + L_X) + 1024);
      glds16_nt(xs + 2048 + (lane < 16 ? lane : 15) * 16, (unsigned)(size_t)(L + L_X) + 2048); }
    v4u wv[9];
    { const u16* p = a.wq + (size_t)(2 * g + (n >> 3)) * 4096 + h * 128 + dh * 64 + 8 * (n & 7);
#pragma unroll
      for (int k = 0; k < 9; ++k) { int I = wave + 8 * k + 18 * cq; I = I >= 72 ? I - 72 : I; wv[k] = ldnt(p + (size_t)I * (8 * 4096)); } }
    __builtin_amdgcn_sched_barrier(0);
    bar();
    __builtin_amdgcn_s_setprio(3);
    {
      if (w == 0) { VMWAIT(9); cnt_add(L, F_X, lane); }
      cnt_wait<1>(L, F_X, 1u);
      unsigned xv[9];
#pragma unroll
      for (int k = 0; k < 9; ++k) { int I = w + 8 * k + 18 * cq; I = I >= 72 ? I - 72 : I; xv[k] = *(const LAS unsigned*)(L + L_X + (n >> 3) * 1152 + (8 * I + 2 * g) * 2); }
      f32x4 a0 = z4, a1 = z4;
#pragma unroll
      for (int k = 0; k < 9; ++k) { a0 = mfma(diag(dg, xv[k] & 0xffffu), wv[k], a0); a1 = mfma(diag(dg, xv[k] >> 16), wv[k], a1); }
      f32x4 v = n < 8 ? a0 : a1;
#pragma unroll
      for (int k = 0; k < 4; ++k) v[k] += dppf<ROR8>(v[k]);
      if (n < 8) *(LAS f32x4*)(L + L_QP + ((w * 2 + (g >> 1)) * 64 + 8 * n + 4 * (g & 1)) * 4) = v;
      flag_set(L, F_QP + w, lane); }
    const u64 ep = __hip_atomic_load((const GAS u64*)(a.ws + WS_EPOCH), __ATOMIC_RELAXED, __HIP_MEMORY_SCOPE_AGENT) + 1ull;
    const unsigned elo = (unsigned)ep, ehi = (unsigned)(ep >> 32);
    __builtin_amdgcn_s_setprio(0);
    cnt_wait<2>(L, F_SENT, 2u);
    flag_wait<4, 2>(L, F_KV);
    __builtin_amdgcn_s_setprio(3);
    v4u kf[16];
    { const int k8 = lane & 7, hq = lane >> 3;
      const int qb = WS_QA + ((cb * 32 + 16 * hgA + hq) * 8 + w) * 384 + k8 * 16, mo = WS_QA + ((cb * 32 + 16 * hgA + n) * 8 + w) * 384 + 256;
      v4u v0, v1, v2, v3; unsigned spins = 0;
      for (;;) {
        asm volatile("" ::: "memory");
        v0 = LD128(qb); v1 = LD128(qb + 128); v2 = LD128(qb + 8 * 3072); v3 = LD128(qb + 8 * 3072 + 128);
        const v4u mt = LD128(mo);
        __builtin_amdgcn_sched_barrier(0);
        if (spins == 0) {
          cnt_add(L, F_GO, lane);
          const int f = ((n & 3) << 2) | (n >> 2);
          const int rowb = L_KV + (16 * w + n) * 1024;
#pragma unroll
          for (int s = 0; s < 16; ++s) kf[s] = *(const LAS v4u*)(L + rowb + (s >> 2) * 256 + (((4 * g + (s & 3)) ^ f) * 16)); }
        __builtin_amdgcn_sched_barrier(0);
        unsigned U = 0, V = 0; cs(v0, U, V); cs(v1, U, V); cs(v2, U, V); cs(v3, U, V);
        if (lane < 16) { U -= mt.x + mt.z - 2u * elo; V -= mt.y + mt.w - 2u * ehi; }
        if ((wsum(U) | wsum(V)) == 0u || ++spins > SPIN_CAP) break;
        __builtin_amdgcn_s_sleep(6);
      }
      auto add2 = [](v4u p, v4u q) { v4u o;
        o.x = pk2(bflo(p.x) + bflo(q.x), bfhi(p.x) + bfhi(q.x)); o.y = pk2(bflo(p.y) + bflo(q.y), bfhi(p.y) + bfhi(q.y));
        o.z = pk2(bflo(p.z) + bflo(q.z), bfhi(p.z) + bfhi(q.z)); o.w = pk2(bflo(p.w) + bflo(q.w), bfhi(p.w) + bfhi(q.w)); return o; };
      const int wb = L_QA + (4 * (w >> 1) + (k8 & 3)) * 1024 + (2 * (w & 1) + (k8 >> 2)) * 256, rot = hq + 4 * (k8 & 3) + 2 * (w & 1);
      *(LAS v4u*)(L + wb + (rot & 15) * 16) = add2(v0, v1);
      *(LAS v4u*)(L + wb + ((rot + 8) & 15) * 16) = add2(v2, v3);
      flag_set(L, F_QA + w, lane);
    }
    STAMP(2);
    flag_wait<8, SPN>(L, F_QA);
    STAMP(3);
    f32x4 sc = z4, sc2 = z4;
#pragma unroll
    for (int hs = 0; hs < 2; ++hs) {
      v4u qf[8];
#pragma unroll
      for (int s = 0; s < 8; ++s) qf[s] = *(const LAS v4u*)(L + L_QA + (8 * hs + s) * 1024 + g * 256 + ((n + 4 * (s & 3) + 2 * (g >> 1)) & 15) * 16);
      __builtin_amdgcn_sched_barrier(0);
#pragma unroll
      for (int s = 0; s < 8; s += 2) { sc = mfma(kf[8 * hs + s], qf[s], sc); sc2 = mfma(kf[8 * hs + s + 1], qf[s + 1], sc2); }
    }
    sc += sc2;
    { float m = fmaxf(fmaxf(sc.x, sc.y), fmaxf(sc.z, sc.w)); m = x32maxf(x16maxf(m));
      if (g == 0) *(volatile LAS float*)(L + L_SM + n * 32 + w * 4) = m;
      flag_set(L, F_SM + w, lane); }
    float mcu;
    { v4u m0, m1;
      flag_wait<8, SPN>(L, F_SM); lds_rd2((unsigned)(size_t)L + L_SM + n * 32, m0, m1);
      STAMP(9);
      const f32x4 f0 = __builtin_bit_cast(f32x4, m0), f1 = __builtin_bit_cast(f32x4, m1);
      mcu = fmaxf(fmaxf(fmaxf(f0.x, f0.y), fmaxf(f0.z, f0.w)), fmaxf(fmaxf(f1.x, f1.y), fmaxf(f1.z, f1.w)));
      const unsigned p01 = pk2(__builtin_amdgcn_exp2f(sc.x - mcu), __builtin_amdgcn_exp2f(sc.y - mcu));
      const unsigned p23 = pk2(__builtin_amdgcn_exp2f(sc.z - mcu), __builtin_amdgcn_exp2f(sc.w - mcu));
      *(volatile LAS v2u*)(L + L_P + (w >> 1) * 1024 + (n + 16 * (2 * (w & 1) + (g >> 1))) * 16 + 8 * (g & 1)) = v2u{p01, p23};
      float l = (bflo(p01) + bfhi(p01)) + (bflo(p23) + bfhi(p23)); l = x32addf(x16addf(l));
      if (g == 0) *(volatile LAS float*)(L + L_SS + n * 32 + w * 4) = l;
      flag_set(L, F_SS + w, lane); }
    { const int q4 = n >> 2, p4 = n & 3;
      const unsigned base = L_KV + (8 * g + q4) * 1024 + (w >> 1) * 256 + ((((8 * (w & 1)) + (p4 >> 1)) ^ ((q4 << 2) | (2 * (g & 1)))) * 16) + 8 * (p4 & 1);
      bf16x8 vf[16];
#pragma unroll
      for (int ks = 0; ks < 4; ++ks)
#pragma unroll
        for (int T = 0; T < 4; ++T) {
          const s16x4 lo = __builtin_amdgcn_ds_read_tr16_b64_v4i16((LAS s16x4*)(L + ((base ^ (unsigned)(T << 5)) + ks * 32768)));
          const s16x4 hi = __builtin_amdgcn_ds_read_tr16_b64_v4i16((LAS s16x4*)(L + ((base ^ (unsigned)((T << 5) | (1 << 4) | (1 << 12))) + ks * 32768)));
          vf[ks * 4 + T] = __builtin_shufflevector(lo, hi, 0, 1, 2, 3, 4, 5, 6, 7); }
      __builtin_amdgcn_sched_barrier(0);
      v4u l0, l1;
      flag_wait<8, SPN>(L, F_SS);
      l0 = *(const LAS v4u*)(L + L_SS + n * 32); l1 = *(const LAS v4u*)(L + L_SS + n * 32 + 16);
      STAMP(10);
      f32x4 o[4] = {z4, z4, z4, z4};
      v4u pf[4];
#pragma unroll
      for (int ks = 0; ks < 4; ++ks) pf[ks] = *(const LAS v4u*)(L + L_P + ks * 1024 + lane * 16);
#pragma unroll
      for (int ks = 0; ks < 4; ++ks)
#pragma unroll
        for (int T = 0; T < 4; ++T) o[T] = __builtin_amdgcn_mfma_f32_16x16x32_bf16(vf[ks * 4 + T], __builtin_bit_cast(bf16x8, pf[ks]), o[T], 0, 0, 0);
      const f32x4 f0 = __builtin_bit_cast(f32x4, l0), f1 = __builtin_bit_cast(f32x4, l1);
      const float lcu = ((f0.x + f0.y) + (f0.z + f0.w)) + ((f1.x + f1.y) + (f1.z + f1.w));
      const int row = ((16 * hgA + n) * 4 + (w >> 1)) * 128 + kb;
      asm volatile("" :: "v"(o[3]));
      unsigned U = 0, V = 0;
#pragma unroll
      for (int T = 0; T < 4; T += 2) {
        unsigned a0 = pk2(o[T].x, o[T].y), a1 = pk2(o[T].z, o[T].w), b0 = pk2(o[T + 1].x, o[T + 1].y), b1 = pk2(o[T + 1].z, o[T + 1].w);
        auto s0 = __builtin_amdgcn_permlane16_swap(a0, b0, false, false); auto s1 = __builtin_amdgcn_permlane16_swap(a1, b1, false, false);
        const v4u ov = {s0[0], s1[0], s0[1], s1[1]};
        ST128(ov, WS_PO + row * 256 + (8 * (w & 1) + 2 * (T + (g & 1)) + (g >> 1)) * 16);
        cs(ov, U, V);
      }
      U = x32addu(x16addu(U)); V = x32addu(x16addu(V));
      const unsigned mb = __float_as_uint(mcu), lb = __float_as_uint(lcu);
      if (g < 2) ST128((g == 0 ? v4u{mb, lb, d2(mb, 0x2B5D71C3u, d2(lb, 0x8F63E917u, U + elo)), d2(mb, 0xD1A34E8Bu, d2(lb, 0x47F92C6Du, V + ehi))} : v4u{elo, ehi, elo, ehi}), WS_PM + (row * 2 + (w & 1)) * 32 + g * 16); }
    cnt_add(L, F_PUB, lane);
    STAMP(4);
    v4u uv[2];
#pragma unroll
    for (int t = 0; t < 2; ++t) uv[t] = ldnt(a.wuv + ((size_t)(128 * cq + 16 * w + 8 * t + 2 * g + (n >> 3)) * 32 + h) * 128 + 64 * dh + 8 * (n & 7));
    __builtin_amdgcn_sched_barrier(0);
    { const int row0 = (h * 4 + cq) * 128 + 8 * w + g;
      v4u p[4], mt[4]; unsigned spins = 0;
      __builtin_amdgcn_s_sleep(SLH2);
      for (;;) {
        asm volatile("" ::: "memory");
#pragma unroll
        for (int t = 0; t < 4; ++t) { const int row = row0 + 4 * (t & 1) + 64 * (t >> 1); p[t] = LD128(WS_PO + row * 256 + n * 16); mt[t] = LD128(WS_PM + (row * 2 + (n >> 3)) * 32); }
        __builtin_amdgcn_sched_barrier(0);
        if (spins == 0) cnt_add(L, F_H2, lane);
        __builtin_amdgcn_sched_barrier(0);
        unsigned U = 0, V = 0, Um = 0, Vm = 0;
#pragma unroll
        for (int t = 0; t < 4; ++t) { cs(p[t], U, V);
          Um = d2(mt[t].x, 0x2B5D71C3u, d2(mt[t].y, 0x8F63E917u, Um - mt[t].z)); Vm = d2(mt[t].x, 0xD1A34E8Bu, d2(mt[t].y, 0x47F92C6Du, Vm - mt[t].w)); }
        if ((n & 7) == 0) { U += Um + 4u * elo; V += Vm + 4u * ehi; }
        if ((wsum(U) | wsum(V)) == 0u || ++spins > SPIN_CAP) break;
        const int to = WS_PM + ((((h * 4 + cq) * 128 + 64 * (lane >> 5) + 8 * w) * 2) * 32) + (lane & 31) * 16;
        for (;;) {
          __builtin_amdgcn_s_sleep(SLT);
          asm volatile("" ::: "memory");
          const v2u tg = LD64(to);
          if (__all((lane & 1) == 0 || (tg.x == elo && tg.y == ehi)) || ++spins > SPIN_CAP) break;
        }
      }
      STAMP(5);
      f32x4 acc = z4; float Mb[2], Lb[2];
#pragma unroll
      for (int b = 0; b < 2; ++b) {
        const float m0 = __uint_as_float(mt[2 * b].x), m1 = __uint_as_float(mt[2 * b + 1].x);
        Mb[b] = x32maxf(x16maxf(fmaxf(m0, m1)));
        const unsigned wp = pk2(__builtin_amdgcn_exp2f(m0 - Mb[b]), __builtin_amdgcn_exp2f(m1 - Mb[b]));
        const bool mine = (n >> 3) == b;
        acc = mfma(diag(dg, mine ? (wp & 0xffffu) : 0u), p[2 * b], acc); acc = mfma(diag(dg, mine ? (wp >> 16) : 0u), p[2 * b + 1], acc);
        Lb[b] = x32addf(x16addf(bflo(wp) * __uint_as_float(mt[2 * b].y) + bfhi(wp) * __uint_as_float(mt[2 * b + 1].y)));
      }
      *(volatile LAS f32x4*)(L + L_CP + ((w * 2 + (g >> 1)) * 128 + 8 * n + 4 * (g & 1)) * 4) = acc;
      if (lane < 2) *(volatile LAS v2u*)(L + L_CS + (lane * 8 + w) * 8) = v2u{__float_as_uint(lane ? Mb[1] : Mb[0]), __float_as_uint(lane ? Lb[1] : Lb[0])};
      flag_set(L, F_CS + w, lane); }
    STAMP(1);
    { const int b = n >> 3; float fk[8];
      { v4u s[4]; unsigned gv;
        do { gv = *(volatile LAS unsigned*)(L + L_G + b * 4); } while (!__all(gv != SENT));
        flag_wait<8, SPN>(L, F_CS);
#pragma unroll
        for (int k = 0; k < 4; ++k) s[k] = *(const LAS v4u*)(L + L_CS + b * 64 + k * 16);
        f32x2 c[16];
        __builtin_amdgcn_sched_barrier(0);
        lds_rd16x2((unsigned)(size_t)L + L_CP + (b * 128 + 16 * w + 2 * g) * 4, c);
        STAMP(8);
        float M = -INFINITY, Ls = 0.f;
#pragma unroll
        for (int k = 0; k < 4; ++k) M = fmaxf(M, fmaxf(__uint_as_float(s[k].x), __uint_as_float(s[k].z)));
#pragma unroll
        for (int k = 0; k < 4; ++k) { fk[2 * k] = __builtin_amdgcn_exp2f(__uint_as_float(s[k].x) - M); fk[2 * k + 1] = __builtin_amdgcn_exp2f(__uint_as_float(s[k].z) - M);
          Ls = fmaf(fk[2 * k], __uint_as_float(s[k].y), fmaf(fk[2 * k + 1], __uint_as_float(s[k].w), Ls)); }
        const float sg = __uint_as_float(gv) * __builtin_amdgcn_rcpf(Ls);
        f32x4 a0 = z4, a1 = z4;
#pragma unroll
        for (int t = 0; t < 2; ++t) { float s0 = 0.f, s1 = 0.f;
#pragma unroll
          for (int k = 0; k < 8; ++k) { s0 = fmaf(fk[k], c[t * 8 + k].x, s0); s1 = fmaf(fk[k], c[t * 8 + k].y, s1); }
          const unsigned sp = pk2(s0 * sg, s1 * sg);
          a0 = mfma(diag(dg, sp & 0xffffu), uv[t], a0); a1 = mfma(diag(dg, sp >> 16), uv[t], a1); }
        f32x4 v = n < 8 ? a0 : a1;
#pragma unroll
        for (int k = 0; k < 4; ++k) v[k] += dppf<ROR8>(v[k]);
        if (n < 8) *(volatile LAS f32x4*)(L + L_XP + ((w * 2 + (g >> 1)) * 64 + 8 * n + 4 * (g & 1)) * 4) = v; } }
    flag_set(L, F_XP + w, lane);
    STAMP(13);
    if (w < 2) {
      flag_wait<8, SPN>(L, F_XP);
      STAMP(14);
      float s = 0.f;
#pragma unroll
      for (int k = 0; k < 8; ++k) s += *(const LAS float*)(L + L_XP + ((k * 2 + w) * 64 + lane) * 4);
      *(volatile LAS unsigned*)(L + L_CTX + (w * 64 + (lane & 3) * 16 + (((lane >> 2) - 4 * cq) & 15)) * 4) = __float_as_uint(s);
      flag_set(L, F_CTX + w, lane);
    }
    STAMP(6);
    if (w >= 2) {
      const int t6 = tid - 128, f = t6 >> 2, pt = t6 & 3, off = ((f / 48) * MD + 12 * (f % 48) + 4 * (pt < 3 ? pt : 0)) * 4;
      __builtin_amdgcn_s_setprio(0);
      flag_wait<7, 1>(L, F_OUT);
      __builtin_amdgcn_s_setprio(3);
      STAMP(14);
      const f32x4 s = *(const LAS f32x4*)(L + L_OT + off) + *(const LAS f32x4*)(L + L_OF + off);
      v4u o = __builtin_bit_cast(v4u, s);
      unsigned U = 0, V = 0; cs(o, U, V); if (pt == 3) { U = elo; V = ehi; }
      U += dppu<XOR1>(U); U += dppu<XOR2>(U); V += dppu<XOR1>(V); V += dppu<XOR2>(V);
      if (pt == 3) o = v4u{U, V, 0u, 0u};
      ST128(o, WS_FO + f * 16384 + wg * 64 + pt * 16);
    }
    STAMP(7);
    if (wg < 96) {
      const int pt = lane & 3;
      v4u v0, v1; unsigned spins = 0;
      if (w < 2) flag_wait<7, 2>(L, F_OUT);
      __builtin_amdgcn_s_sleep(SLH3);
      for (;;) {
        asm volatile("" ::: "memory");
        v0 = LD128(WS_FO + wg * 16384 + tid * 16); v1 = LD128(WS_FO + wg * 16384 + 8192 + tid * 16);
        unsigned U = 0, V = 0; cs(v0, U, V); cs(v1, U, V);
        if (pt == 3) { U = 2u * elo - v0.x - v1.x; V = 2u * ehi - v0.y - v1.y; }
        if ((wsum(U) | wsum(V)) == 0u || ++spins > SPIN_CAP) break;
        __builtin_amdgcn_s_sleep(4);
      }
      f32x4 s = __builtin_bit_cast(f32x4, v0) + __builtin_bit_cast(f32x4, v1);
#pragma unroll
      for (int k = 0; k < 4; ++k) { float t = s[k]; t += dppf<ROR4>(t); t += dppf<ROR8>(t); t = x32addf(x16addf(t)); s[k] = t; }
      if (lane < 3) *(volatile LAS f32x4*)(L + L_FIN + w * 64 + lane * 16) = s;
      flag_set(L, F_FIN + w, lane);
      STAMP(11);
      if (w == 0) {
        const int b = wg / 48, col = 12 * (wg % 48) + (lane < 12 ? lane : 0);
        const float bb = (1.f - *(const LAS float*)(L + L_G + b * 4)) * bf1(a.b2[col]);
        flag_wait<8, SPN>(L, F_FIN);
        if (lane < 12) {
          float t = bb;
#pragma unroll
          for (int k = 0; k < 8; ++k) t += *(const LAS float*)(L + L_FIN + k * 64 + lane * 4);
          a.out[b * MD + col] = (u16)bfr(t);
          if (wg == 0 && lane == 0) __hip_atomic_store((GAS u64*)(a.ws + WS_EPOCH), ep, __ATOMIC_RELAXED, __HIP_MEMORY_SCOPE_AGENT);
        }
        STAMP(12);
      }
    }
  } else if (wave < 10) {
    const int kw = wave - 8;
    const u64 ep = __hip_atomic_load((const GAS u64*)(a.ws + WS_EPOCH), __ATOMIC_RELAXED, __HIP_MEMORY_SCOPE_AGENT) + 1ull;
    const unsigned elo = (unsigned)ep, ehi = (unsigned)(ep >> 32);
    { v4u uk[8];
#pragma unroll
      for (int rg = 0; rg < 8; ++rg) uk[rg] = ldnt(a.wuk + ((size_t)(128 * cq + 64 * kw + 8 * (n & 7) + rg) * 32 + h) * 128 + 64 * dh + 32 * (n >> 3) + 8 * g);
      __builtin_amdgcn_sched_barrier(0);
      bar();
      __builtin_amdgcn_s_setprio(3);
      STAMP(8);
      flag_wait<8, SPN>(L, F_QP);
      STAMP(10);
      { f32x2 s = {0.f, 0.f};
#pragma unroll
        for (int k = 0; k < 8; ++k) s += *(const LAS f32x2*)(L + L_QP + ((k * 2 + (lane >> 5)) * 64 + 2 * (lane & 31)) * 4);
        *(volatile LAS unsigned*)(L + L_QB + kw * 256 + lane * 4) = pk2(s.x, s.y); }
      v4u qa0 = lds_v4(L, L_QB + kw * 256 + (n & 1) * 128 + g * 16), qa1 = lds_v4(L, L_QB + kw * 256 + (n & 1) * 128 + 64 + g * 16);
      if (n >= 2) { qa0 = v4u{0u, 0u, 0u, 0u}; qa1 = qa0; }
      constexpr float SC = 0.08838834764831845f * 1.4426950408889634f;
      float sel[8];
#pragma unroll
      for (int rg = 0; rg < 8; ++rg) {
        const f32x4 a1 = mfma(qa0, uk[rg], z4), a2 = mfma(qa1, uk[rg], z4);
        float v0 = n < 8 ? a1.x : a2.x, v1 = n < 8 ? a1.y : a2.y;
        v0 += dppf<ROR8>(v0); v1 += dppf<ROR8>(v1);
        sel[rg] = (n < 8 ? v0 : v1) * SC;
      }
      const v4u o = {pk2(sel[0], sel[1]), pk2(sel[2], sel[3]), pk2(sel[4], sel[5]), pk2(sel[6], sel[7])};
      const int qb = WS_QA + (((n >> 3) * 32 + h) * 8 + 2 * cq + kw) * 384;
      if (lane < 16) ST128(o, qb + dh * 128 + (n & 7) * 16);
      unsigned U = 0, V = 0; cs(o, U, V);
      U += dppu<XOR1>(U); U += dppu<XOR2>(U); U += dppu<HMIR>(U); V += dppu<XOR1>(V); V += dppu<XOR2>(V); V += dppu<HMIR>(V);
      if (lane < 16 && (n & 7) == 0) ST64((v2u{U + elo, V + ehi}), qb + 256 + dh * 8);
      cnt_add(L, F_SENT, lane); }
    __builtin_amdgcn_s_setprio(0);
    STAMP(1);
    cnt_wait<4>(L, F_PUB, 8u);
    f32x4 acc[3] = {z4, z4, z4};
#pragma unroll
    for (int pass = 0; pass < 2; ++pass) {
      const int it = wg + 256 * pass;
      if (pass == 1 && wg >= 32) break;
      const int hb = L_HID + pass * 128;
      v4u w2v[12];
      const u16* wp = a.w2 + (size_t)(16 * it + g) * MD + 384 * kw;
#pragma unroll
      for (int t = 0; t < 4; ++t) { w2v[t] = ldnt(wp + (size_t)4 * t * MD + 8 * n); w2v[4 + t] = ldnt(wp + (size_t)4 * t * MD + 128 + 8 * (kw ? (n & 7) : n)); }
      if (kw == 0) {
#pragma unroll
        for (int t = 0; t < 4; ++t) w2v[8 + t] = ldnt(wp + (size_t)4 * t * MD + 256 + 8 * n);
        const v2u b1p = *(const v2u*)(a.b1 + 16 * it + (n & 1) * 8 + 4 * (g & 1));
        v4u v[6]; unsigned spins = 0;
        for (;;) {
          asm volatile("" ::: "memory");
#pragma unroll
          for (int k = 0; k < 6; ++k) v[k] = LD128(WS_HX + ((it >> 3) * 6 + k) * 1152 + lane * 16);
          const v2u mt = LD64(WS_HX + ((it >> 3) * 6 + (lane < 6 ? lane : 0)) * 1152 + 1024);
          unsigned U = 0, V = 0;
#pragma unroll
          for (int k = 0; k < 6; ++k) cs(v[k], U, V);
          if (lane < 6) { U -= mt.x - elo; V -= mt.y - ehi; }
          if ((wsum(U) | wsum(V)) == 0u || ++spins > SPIN_CAP) break;
          __builtin_amdgcn_s_sleep(20);
        }
        unsigned gv; do { gv = *(volatile LAS unsigned*)(L + L_G + (g >> 1) * 4); } while (!__all(gv != SENT));
        f32x4 s = z4;
#pragma unroll
        for (int k = 0; k < 6; ++k) s += __builtin_bit_cast(f32x4, v[k]);
        if ((n >> 1) == (it & 7)) {
          const float og = 1.f - __uint_as_float(gv);
          f32x4 hv; hv.x = fmaxf(s.x + bflo(b1p.x), 0.f); hv.y = fmaxf(s.y + bfhi(b1p.x), 0.f); hv.z = fmaxf(s.z + bflo(b1p.y), 0.f); hv.w = fmaxf(s.w + bfhi(b1p.y), 0.f);
          *(volatile LAS f32x4*)(L + hb + ((g >> 1) * 16 + 8 * (n & 1) + 4 * (g & 1)) * 4) = hv * hv * og;
        }
        cnt_add(L, F_HID, lane);
      } else cnt_wait<4>(L, F_HID, (unsigned)pass + 1u);
#pragma unroll
      for (int t = 0; t < 4; ++t) { const v4u d = diag(dg, bfr(*(volatile LAS float*)(L + hb + ((n >> 3) * 16 + 4 * t + g) * 4)));
        acc[0] = mfma(d, w2v[t], acc[0]); acc[1] = mfma(d, w2v[4 + t], acc[1]); if (kw == 0) acc[2] = mfma(d, w2v[8 + t], acc[2]); }
    }
    { const int ob = L_OF + ((g >> 1) * MD + 384 * kw + 8 * n + 4 * (g & 1)) * 4;
      *(volatile LAS f32x4*)(L + ob) = acc[0];
      if (kw == 0 || n < 8) *(volatile LAS f32x4*)(L + ob + 512) = acc[1];
      if (kw == 0) *(volatile LAS f32x4*)(L + ob + 1024) = acc[2]; }
    flag_set(L, F_OUT + (5 + kw), lane);
    STAMP(3);
  } else if (wave < 15) {
    const int T = wave - 10;
    if (wave == 14) {
      const unsigned char* cp = (const unsigned char*)(pc0 & ~127ull) + (r * 8 + (lane & 7)) * 128;
      if (lane < 8 && (r * 8 + lane) * 128 < CODEPF) { const unsigned v = *(const volatile GAS unsigned*)cp; asm volatile("" :: "v"(v)); }
    } else if (wave >= 12) { const int t2 = tid - 768; if (t2 < (L_S1 - L_S0) / 16) *(LAS v4u*)(L + L_S0 + t2 * 16) = v4u{SENT, SENT, SENT, SENT}; }
    bar();
    const u16* p = a.wo + (size_t)(h * 128 + 64 * dh) * MD;
    if (T < 4) {
      { const unsigned char* src = (const unsigned char*)(a.ckv + (size_t)kb * 128 * 512);
#pragma unroll 1
        for (int i = T; i < 128; i += 4) {
          const int row = (i + 64 * hgA) & 127;
          const int f = ((row & 3) << 2) | ((row >> 2) & 3);
          glds16_nt(src + row * 1024 + ((lane ^ f) * 16), (unsigned)(size_t)(L + L_KV) + row * 1024);
          if (i < 4 * NTH) asm volatile("s_waitcnt vmcnt(%0)" :: "n"(WKV - 1) : "memory");
        } }
      cnt_add(L, F_DMAI, lane);
      STAMP(8);
      VMWAIT(0);
      flag_set(L, F_KV + T, lane);
      STAMP(1);
      cnt_wait<2>(L, F_GO, 8u);
      v4u wo[16];
#pragma unroll
      for (int t = 0; t < 16; ++t) {
        if (t == ST1) { __builtin_amdgcn_sched_barrier(0); cnt_wait<3>(L, F_PUB, 8u); }
        if (t == ST2) { __builtin_amdgcn_sched_barrier(0); cnt_wait<3>(L, F_H2, 8u); }
        wo[t] = ldnt(p + (size_t)(4 * ((t + 4 * cq) & 15) + g) * MD + 128 * T + 8 * n); }
      __builtin_amdgcn_sched_barrier(0);
      STAMP(2);
      flag_wait<1, 4>(L, F_CS);
      __builtin_amdgcn_s_setprio(3);
      f32x4 acc = z4; v4u c[4];
      flag_wait<2, SPN>(L, F_CTX);
      lds_rd4((unsigned)(size_t)L + L_CTX + ((n >> 3) * 64 + g * 16) * 4, c[0], c[1], c[2], c[3]);
      STAMP(9);
#pragma unroll
      for (int t4 = 0; t4 < 4; ++t4) { const unsigned c01 = pk2(__uint_as_float(c[t4].x), __uint_as_float(c[t4].y)), c23 = pk2(__uint_as_float(c[t4].z), __uint_as_float(c[t4].w));
        acc = mfma(diag(dg, c01 & 0xffffu), wo[4 * t4], acc); acc = mfma(diag(dg, c01 >> 16), wo[4 * t4 + 1], acc);
        acc = mfma(diag(dg, c23 & 0xffffu), wo[4 * t4 + 2], acc); acc = mfma(diag(dg, c23 >> 16), wo[4 * t4 + 3], acc); }
      *(volatile LAS f32x4*)(L + L_OT + ((g >> 1) * MD + 128 * T + 8 * n + 4 * (g & 1)) * 4) = acc;
    } else {
      cnt_wait<4>(L, F_PUB, 8u);
      v4u wo[8];
#pragma unroll
      for (int t = 0; t < 8; ++t) wo[t] = ldnt(p + (size_t)(8 * ((t + 2 * cq) & 7) + 2 * g + (n >> 3)) * MD + 512 + 8 * (n & 7));
      __builtin_amdgcn_sched_barrier(0);
      flag_wait<1, 4>(L, F_CS);
      __builtin_amdgcn_s_setprio(3);
      v4u c[8];
      flag_wait<2, SPN>(L, F_CTX);
      lds_rd8((unsigned)(size_t)L + L_CTX + ((n >> 3) * 64 + (2 * (g & 1)) * 16) * 4, c);
      f32x4 a0 = z4, a1 = z4;
#pragma unroll
      for (int t = 0; t < 8; ++t) { const int e = 2 * (t & 1);
        const unsigned u0 = (g >> 1) ? c[t >> 1][e + 1] : c[t >> 1][e], u1 = (g >> 1) ? c[4 + (t >> 1)][e + 1] : c[4 + (t >> 1)][e];
        const unsigned cp = pk2(__uint_as_float(u0), __uint_as_float(u1));
        a0 = mfma(diag(dg, cp & 0xffffu), wo[t], a0); a1 = mfma(diag(dg, cp >> 16), wo[t], a1); }
      f32x4 v = n < 8 ? a0 : a1;
#pragma unroll
      for (int k = 0; k < 4; ++k) v[k] += dppf<ROR8>(v[k]);
      if (n < 8) *(volatile LAS f32x4*)(L + L_OT + ((g >> 1) * MD + 512 + 8 * n + 4 * (g & 1)) * 4) = v;
    }
    flag_set(L, F_OUT + T, lane);
    STAMP(3);
  } else {
    bar();
    cnt_wait<2>(L, F_X, 1u);
    const u64 ep = __hip_atomic_load((const GAS u64*)(a.ws + WS_EPOCH), __ATOMIC_RELAXED, __HIP_MEMORY_SCOPE_AGENT) + 1ull;
    const unsigned elo = (unsigned)ep, ehi = (unsigned)(ep >> 32);
    { float s0 = 0.f, s1 = 0.f;
      if (lane < 36) {
        const v4u gw = *(const v4u*)(a.gw + 8 * lane), gw2 = *(const v4u*)(a.gw + 288 + 8 * lane);
        const v4u xa = *(const LAS v4u*)(L + L_X + 16 * lane), xa2 = *(const LAS v4u*)(L + L_X + 576 + 16 * lane), xb = *(const LAS v4u*)(L + L_X + 1152 + 16 * lane), xb2 = *(const LAS v4u*)(L + L_X + 1728 + 16 * lane);
#pragma unroll
        for (int k = 0; k < 4; ++k) {
          s0 += bflo(gw[k]) * bflo(xa[k]) + bfhi(gw[k]) * bfhi(xa[k]) + bflo(gw2[k]) * bflo(xa2[k]) + bfhi(gw2[k]) * bfhi(xa2[k]);
          s1 += bflo(gw[k]) * bflo(xb[k]) + bfhi(gw[k]) * bfhi(xb[k]) + bflo(gw2[k]) * bflo(xb2[k]) + bfhi(gw2[k]) * bfhi(xb2[k]); }
      }
      s0 = wsumf(s0); s1 = wsumf(s1);
      if (lane < 2) *(volatile LAS unsigned*)(L + L_G + lane * 4) = min(__float_as_uint(1.f / (1.f + __expf(-(lane ? s1 : s0)))), SENT - 1u); }
    if (wg < 216) {
      const int T = wg / 6, rg = wg % 6;
      cnt_wait<4>(L, F_DMAI, 4u);
      const u16* p = a.w1 + (size_t)(96 * rg + 24 * g) * NF + 128 * T + 8 * n;
      v4u wv[24];
#pragma unroll
      for (int t = 0; t < 24; ++t) wv[t] = ldnt(p + (size_t)t * NF);
      __builtin_amdgcn_sched_barrier(0);
      v4u xs[3];
#pragma unroll
      for (int k = 0; k < 3; ++k) xs[k] = *(const LAS v4u*)(L + L_X + (n >> 3) * 1152 + (96 * rg + 24 * g) * 2 + 16 * k);
      f32x4 acc = z4;
#pragma unroll
      for (int t = 0; t < 24; ++t) { const unsigned xd = xs[t >> 3][(t >> 1) & 3]; acc = mfma(diag(dg, (t & 1) ? (xd >> 16) : (xd & 0xffffu)), wv[t], acc); }
      const v4u o = __builtin_bit_cast(v4u, acc);
      ST128(o, WS_HX + wg * 1152 + lane * 16);
      unsigned U = 0, V = 0; cs(o, U, V); U = wsum(U); V = wsum(V);
      if (lane == 0) ST64((v2u{U + elo, V + ehi}), WS_HX + wg * 1152 + 1024);
      STAMP(3);
    }
  }
  STAMP_OUT();
}
}

extern "C" void kernel_launch(void* const* d_in, const int* in_sizes, int n_in, void* d_out, int out_size, void* d_ws, size_t ws_size, hipStream_t stream) {
  Args a;
  a.x = (const u16*)d_in[0]; a.ckv = (const u16*)d_in[1]; a.wq = (const u16*)d_in[2]; a.wuk = (const u16*)d_in[3]; a.wuv = (const u16*)d_in[4]; a.wo = (const u16*)d_in[5];
  a.w1 = (const u16*)d_in[7]; a.b1 = (const u16*)d_in[8]; a.w2 = (const u16*)d_in[9]; a.b2 = (const u16*)d_in[10]; a.gw = (const u16*)d_in[11];
  a.out = (u16*)d_out; a.ws = (unsigned char*)d_ws;
  (void)hipFuncSetAttribute((const void*)fused_kernel, hipFuncAttributeMaxDynamicSharedMemorySize, L_END);
  hipLaunchKernelGGL(fused_kernel, dim3(256), dim3(1024), L_END, stream, a);
}
```

```cpp
#define PROBE 0
#include <hip/hip_runtime.h>
#include <hip/hip_bf16.h>
#include <stdint.h>

typedef unsigned short u16;
typedef unsigned long long u64;
typedef unsigned v4u __attribute__((ext_vector_type(4)));
typedef unsigned v2u __attribute__((ext_vector_type(2)));
typedef float f32x4 __attribute__((ext_vector_type(4)));
typedef float f32x2 __attribute__((ext_vector_type(2)));
typedef short bf16x8 __attribute__((ext_vector_type(8)));
typedef short s16x4 __attribute__((ext_vector_type(4)));
#define LAS __attribute__((address_space(3)))
#define GAS __attribute__((address_space(1)))
#define DI __device__ __forceinline__

namespace {
constexpr int MD = 576, NF = 4608;
constexpr int WS_EPOCH = 0;
constexpr int WS_QA = 4096;
constexpr int WS_HX = WS_QA + 512 * 384;
constexpr int WS_PM = WS_HX + 216 * 1152;
constexpr int WS_PO = WS_PM + 128 * 8192;
constexpr int WS_FO = WS_PO + 128 * 32768;
constexpr int WS_END = WS_FO + 96 * 16384;
constexpr int WS_STAMP = WS_END + 4096;
constexpr int L_KV = 0;
constexpr int L_QA = 131072;
constexpr int L_CP = L_QA, L_XP = L_QA + 8192;
constexpr int L_P = L_QA + 16384;
constexpr int L_QP = L_P;
constexpr int L_OT = L_KV;
constexpr int L_OF = L_P + 4096;
constexpr int L_X = L_OF + 4608;
constexpr int L_QB = L_X + 3072;
constexpr int L_HID = L_QB + 512;
constexpr int L_S0 = L_HID + 256;
constexpr int L_SM = L_S0;
constexpr int L_SS = L_SM + 512;
constexpr int L_CS = L_SS + 512;
constexpr int L_CTX = L_CS + 128;
constexpr int L_G = L_CTX + 512;
constexpr int L_FL = L_G + 16;
constexpr int F_QP = L_FL, F_QA = L_FL + 8, F_KV = L_FL + 16, F_XP = L_FL + 24, F_OUT = L_FL + 32, F_FIN = L_FL + 40, F_SM = L_FL + 48, F_SS = L_FL + 56, F_CS = L_FL + 64, F_CTX = L_FL + 72, F_MISC = L_FL + 192;
constexpr int F_X = F_MISC, F_ISS = F_MISC + 4, F_SENT = F_MISC + 8, F_DMAI = F_MISC + 12, F_HID = F_MISC + 16, F_GO = F_MISC + 20, F_PUB = F_MISC + 24, F_H2 = F_MISC + 28;
constexpr int L_S1 = L_FL + 224;
constexpr int L_FIN = L_S1;
constexpr int L_END = L_FIN + 512;
static_assert(L_END <= 163840 && (L_S1 - L_S0) % 16 == 0 && (L_S1 - L_S0) / 16 <= 128, "lds");
#define SPINCAP (1u << 18)
#define GOEARLY 1
#define SPN 0
#define SLT 3
#define ST1 12
#define ST2 16
#define CODEPF 28000
#define WKV 5
#define NTH 8
#define SLH1 4
#define SLH2 36
#define SLH3 16
constexpr unsigned SPIN_CAP = SPINCAP, SENT = 0xFFFFFFFFu;
constexpr int AUX_SC1 = 16;

struct Args {
  const u16 *x, *ckv, *wq, *wuk, *wuv, *wo, *w1, *b1, *w2, *b2, *gw;
  u16* out;
  unsigned char* ws;
};

DI void bar() { asm volatile("s_waitcnt lgkmcnt(0)" ::: "memory"); __builtin_amdgcn_s_barrier(); asm volatile("" ::: "memory"); }
DI void lds_wait(LAS unsigned char* L, int off, unsigned want) {
  while (__hip_atomic_load((LAS unsigned*)(L + off), __ATOMIC_RELAXED, __HIP_MEMORY_SCOPE_WORKGROUP) < want) __builtin_amdgcn_s_sleep(1);
  asm volatile("" ::: "memory");
}
DI void lds_wait_fast(LAS unsigned char* L, int off, unsigned want) {
  while (__hip_atomic_load((LAS unsigned*)(L + off), __ATOMIC_RELAXED, __HIP_MEMORY_SCOPE_WORKGROUP) < want) { }
  asm volatile("" ::: "memory");
}
DI void lds_wait_slow(LAS unsigned char* L, int off, unsigned want) {
  while (__hip_atomic_load((LAS unsigned*)(L + off), __ATOMIC_RELAXED, __HIP_MEMORY_SCOPE_WORKGROUP) < want) __builtin_amdgcn_s_sleep(4);
  asm volatile("" ::: "memory");
}
DI void lds_set(LAS unsigned char* L, int off, int lane) {
  asm volatile("s_waitcnt lgkmcnt(0)" ::: "memory");
  if (lane == 0) (void)__hip_atomic_fetch_add((LAS unsigned*)(L + off), 1u, __ATOMIC_RELAXED, __HIP_MEMORY_SCOPE_WORKGROUP);
}
DI v4u ldnt(const void* p) { return *(const GAS v4u*)p; }
DI f32x4 mfma(v4u a, v4u b, f32x4 c) { return __builtin_amdgcn_mfma_f32_16x16x32_bf16(__builtin_bit_cast(bf16x8, a), __builtin_bit_cast(bf16x8, b), c, 0, 0, 0); }
DI float bflo(unsigned u) { return __uint_as_float(u << 16); }
DI float bfhi(unsigned u) { return __uint_as_float(u & 0xffff0000u); }
DI float bf1(u16 v) { return __uint_as_float(((unsigned)v) << 16); }
DI unsigned pk2(float lo, float hi) {
  typedef __bf16 bf2 __attribute__((ext_vector_type(2)));
  bf2 r; r.x = (__bf16)lo; r.y = (__bf16)hi; return __builtin_bit_cast(unsigned, r);
}
DI unsigned rotl(unsigned x, unsigned r) { return __builtin_amdgcn_alignbit(x, x, 32u - r); }
template <int CTRL> DI unsigned dppu(unsigned x) { return (unsigned)__builtin_amdgcn_mov_dpp((int)x, CTRL, 0xf, 0xf, true); }
template <int CTRL> DI float dppf(float x) { return __uint_as_float(dppu<CTRL>(__float_as_uint(x))); }
constexpr int XOR1 = 0xB1, XOR2 = 0x4E, HMIR = 0x141, ROR4 = 0x124, ROR8 = 0x128;
DI unsigned x16addu(unsigned x) { auto s = __builtin_amdgcn_permlane16_swap(x, x, false, false); return s[0] + s[1]; }
DI unsigned x32addu(unsigned x) { auto s = __builtin_amdgcn_permlane32_swap(x, x, false, false); return s[0] + s[1]; }
DI float x16addf(float x) { auto s = __builtin_amdgcn_permlane16_swap(__float_as_uint(x), __float_as_uint(x), false, false); return __uint_as_float(s[0]) + __uint_as_float(s[1]); }
DI float x32addf(float x) { auto s = __builtin_amdgcn_permlane32_swap(__float_as_uint(x), __float_as_uint(x), false, false); return __uint_as_float(s[0]) + __uint_as_float(s[1]); }
DI float x16maxf(float x) { auto s = __builtin_amdgcn_permlane16_swap(__float_as_uint(x), __float_as_uint(x), false, false); return fmaxf(__uint_as_float(s[0]), __uint_as_float(s[1])); }
DI float x32maxf(float x) { auto s = __builtin_amdgcn_permlane32_swap(__float_as_uint(x), __float_as_uint(x), false, false); return fmaxf(__uint_as_float(s[0]), __uint_as_float(s[1])); }
DI unsigned wsum(unsigned x) { x += dppu<XOR1>(x); x += dppu<XOR2>(x); x += dppu<ROR4>(x); x += dppu<ROR8>(x); return x32addu(x16addu(x)); }
DI float wsumf(float x) { x += dppf<XOR1>(x); x += dppf<XOR2>(x); x += dppf<ROR4>(x); x += dppf<ROR8>(x); return x32addf(x16addf(x)); }
DI unsigned d2(unsigned a, unsigned k, unsigned c) { typedef unsigned short us2 __attribute__((ext_vector_type(2)));
  return __builtin_amdgcn_udot2(__builtin_bit_cast(us2, a), __builtin_bit_cast(us2, k), c, false); }
DI void cs(v4u a, unsigned& U, unsigned& V) {
  U = d2(a.x, 0x9E37B5C5u, U); U = d2(a.y, 0x7A4DC2B3u, U); U = d2(a.z, 0xE4F1594Du, U); U = d2(a.w, 0x3B8FD16Fu, U);
  V = d2(a.x, 0x62E9F2A7u, V); V = d2(a.y, 0xC96B4F1Du, V); V = d2(a.z, 0x5573A9E5u, V); V = d2(a.w, 0xED2F8799u, V);
}
DI v4u lds_v4(LAS unsigned char* L, int off) { return *(volatile LAS v4u*)(L + off); }
DI void lds_rd2(unsigned ad, v4u& a, v4u& b) { asm volatile("ds_read_b128 %0, %2\n\tds_read_b128 %1, %2 offset:16\n\ts_waitcnt lgkmcnt(0)" : "=&v"(a), "=&v"(b) : "v"(ad) : "memory"); }
DI void lds_rd4(unsigned ad, v4u& a, v4u& b, v4u& c, v4u& d) {
  asm volatile("ds_read_b128 %0, %4\n\tds_read_b128 %1, %4 offset:16\n\tds_read_b128 %2, %4 offset:32\n\tds_read_b128 %3, %4 offset:48\n\ts_waitcnt lgkmcnt(0)" : "=&v"(a), "=&v"(b), "=&v"(c), "=&v"(d) : "v"(ad) : "memory"); }
DI void lds_rd8(unsigned ad, v4u (&c)[8]) {
  asm volatile("ds_read_b128 %0, %8\n\tds_read_b128 %1, %8 offset:16\n\tds_read_b128 %2, %8 offset:32\n\tds_read_b128 %3, %8 offset:48\n\t"
               "ds_read_b128 %4, %8 offset:64\n\tds_read_b128 %5, %8 offset:80\n\tds_read_b128 %6, %8 offset:96\n\tds_read_b128 %7, %8 offset:112\n\ts_waitcnt lgkmcnt(0)"
               : "=&v"(c[0]), "=&v"(c[1]), "=&v"(c[2]), "=&v"(c[3]), "=&v"(c[4]), "=&v"(c[5]), "=&v"(c[6]), "=&v"(c[7]) : "v"(ad) : "memory"); }
DI void lds_rd16x2(unsigned ad, f32x2 (&c)[16]) {
  asm volatile("ds_read_b64 %0, %16\n\tds_read_b64 %1, %16 offset:1024\n\tds_read_b64 %2, %16 offset:2048\n\tds_read_b64 %3, %16 offset:3072\n\t"
               "ds_read_b64 %4, %16 offset:4096\n\tds_read_b64 %5, %16 offset:5120\n\tds_read_b64 %6, %16 offset:6144\n\tds_read_b64 %7, %16 offset:7168\n\t"
               "ds_read_b64 %8, %16 offset:32\n\tds_read_b64 %9, %16 offset:1056\n\tds_read_b64 %10, %16 offset:2080\n\tds_read_b64 %11, %16 offset:3104\n\t"
               "ds_read_b64 %12, %16 offset:4128\n\tds_read_b64 %13, %16 offset:5152\n\tds_read_b64 %14, %16 offset:6176\n\tds_read_b64 %15, %16 offset:7200\n\ts_waitcnt lgkmcnt(0)"
               : "=&v"(c[0]), "=&v"(c[1]), "=&v"(c[2]), "=&v"(c[3]), "=&v"(c[4]), "=&v"(c[5]), "=&v"(c[6]), "=&v"(c[7]),
                 "=&v"(c[8]), "=&v"(c[9]), "=&v"(c[10]), "=&v"(c[11]), "=&v"(c[12]), "=&v"(c[13]), "=&v"(c[14]), "=&v"(c[15]) : "v"(ad) : "memory"); }
DI bool nos(v4u v) { return (v.x != SENT) & (v.y != SENT) & (v.z != SENT) & (v.w != SENT); }
template <int N, int SLEEP> DI void flag_wait(LAS unsigned char* L, int off) {
  constexpr u64 MASK = N == 8 ? ~0ull : ((1ull << (8 * (N & 7))) - 1ull);
  while ((*(volatile LAS u64*)(L + off) & MASK) != 0ull) { if (SLEEP) __builtin_amdgcn_s_sleep(SLEEP); }
  asm volatile("" ::: "memory");
}
DI void flag_set(LAS unsigned char* L, int off, int lane) { asm volatile("" ::: "memory"); if (lane == 0) *(volatile LAS unsigned char*)(L + off) = 0; asm volatile("" ::: "memory"); }
template <int SLEEP> DI void cnt_wait(LAS unsigned char* L, int off, unsigned n) {
  while (*(volatile LAS unsigned*)(L + off) + 1u < n) { if (SLEEP) __builtin_amdgcn_s_sleep(SLEEP); }
  asm volatile("" ::: "memory");
}
DI void cnt_add(LAS unsigned char* L, int off, int lane) { asm volatile("" ::: "memory"); if (lane == 0) (void)__hip_atomic_fetch_add((LAS unsigned*)(L + off), 1u, __ATOMIC_RELAXED, __HIP_MEMORY_SCOPE_WORKGROUP); asm volatile("" ::: "memory"); }
struct Diag { unsigned m0, m1, m2, m3, sh; };
DI Diag make_diag(int i) { const int p = i & 7; Diag d; d.sh = (p & 1) * 16; d.m0 = (p >> 1) == 0 ? ~0u : 0u; d.m1 = (p >> 1) == 1 ? ~0u : 0u; d.m2 = (p >> 1) == 2 ? ~0u : 0u; d.m3 = (p >> 1) == 3 ? ~0u : 0u; return d; }
DI v4u diag(const Diag& d, unsigned vb16) { const unsigned v = vb16 << d.sh; return v4u{v & d.m0, v & d.m1, v & d.m2, v & d.m3}; }
DI unsigned bfr(float f) { return pk2(f, 0.f) & 0xffffu; }
DI void glds16_nt(const void* gsrc, unsigned lds_dst) {
  unsigned keep;
  asm volatile("s_mov_b32 %0, m0\n\ts_mov_b32 m0, %2\n\ts_nop 0\n\tglobal_load_lds_dwordx4 %1, off\n\ts_mov_b32 m0, %0" : "=&s"(keep) : "v"(gsrc), "s"(lds_dst) : "memory");
}
#define LD128(off) __builtin_amdgcn_raw_buffer_load_b128(rs, (off), 0, AUX_SC1)
#define LD64(off) __builtin_amdgcn_raw_buffer_load_b64(rs, (off), 0, AUX_SC1)
#define ST128(v, off) __builtin_amdgcn_raw_buffer_store_b128((v), rs, (off), 0, AUX_SC1)
#define ST64(v, off) __builtin_amdgcn_raw_buffer_store_b64((v), rs, (off), 0, AUX_SC1)
#define VMWAIT(N) do { __builtin_amdgcn_sched_barrier(0); asm volatile("s_waitcnt vmcnt(%0)" :: "n"(N) : "memory"); __builtin_amdgcn_sched_barrier(0); } while (0)
#define STAMP(k) do { } while (0)
#define STAMP_OUT() do { } while (0)

__global__ void __launch_bounds__(1024) fused_kernel(Args a) {
  u64 pc0; asm volatile("s_getpc_b64 %0" : "=s"(pc0));
  extern __shared__ __attribute__((aligned(16))) unsigned char smem[];
  LAS unsigned char* L = (LAS unsigned char*)smem;
  const int tid = threadIdx.x, lane = tid & 63, wave = __builtin_amdgcn_readfirstlane(tid >> 6);
  const int wg = blockIdx.x, xl = wg & 7, r = wg >> 3;
  const int n = lane & 15, g = lane >> 4;
  const int h = xl + 8 * (r >> 3), j = r & 7, cq = j >> 1, dh = j & 1;
  const int hgA = r >> 4, kb = xl * 16 + (r & 15), cb = kb >> 6;
  const __amdgpu_buffer_rsrc_t rs = __builtin_amdgcn_make_buffer_rsrc(a.ws, 0, WS_END, 0x00020000);
  const Diag dg = make_diag(n);
  const f32x4 z4 = {0.f, 0.f, 0.f, 0.f};
  if (tid == 960 && wg < 256) __hip_atomic_store((GAS unsigned*)(a.ws + WS_STAMP + wg * 4), (unsigned)__builtin_amdgcn_s_memrealtime(), __ATOMIC_RELAXED, __HIP_MEMORY_SCOPE_AGENT);
  if (__builtin_expect(wave < 8, 1)) {
    const int w = wave;
    if (w == 0) { const unsigned char* xs = (const unsigned char*)a.x;
      glds16_nt(xs + lane * 16, (unsigned)(size_t)(L + L_X)); glds16_nt(xs + 1024 + lane * 16, (unsigned)(size_t)(L + L_X) + 1024);
      glds16_nt(xs + 2048 + (lane < 16 ? lane : 15) * 16, (unsigned)(size_t)(L + L_X) + 2048); }
    v4u wv[9];
    { const u16* p = a.wq + (size_t)(2 * g + (n >> 3)) * 4096 + h * 128 + dh * 64 + 8 * (n & 7);
#pragma unroll
      for (int k = 0; k < 9; ++k) { int I = wave + 8 * k + 18 * cq; I = I >= 72 ? I - 72 : I; wv[k] = ldnt(p + (size_t)I * (8 * 4096)); } }
    __builtin_amdgcn_sched_barrier(0);
    bar();
    __builtin_amdgcn_s_setprio(3);
    {
      if (w == 0) { VMWAIT(9); cnt_add(L, F_X, lane); }
      cnt_wait<1>(L, F_X, 1u);
      unsigned xv[9];
#pragma unroll
      for (int k = 0; k < 9; ++k) { int I = w + 8 * k + 18 * cq; I = I >= 72 ? I - 72 : I; xv[k] = *(const LAS unsigned*)(L + L_X + (n >> 3) * 1152 + (8 * I + 2 * g) * 2); }
      f32x4 a0 = z4, a1 = z4;
#pragma unroll
      for (int k = 0; k < 9; ++k) { a0 = mfma(diag(dg, xv[k] & 0xffffu), wv[k], a0); a1 = mfma(diag(dg, xv[k] >> 16), wv[k], a1); }
      f32x4 v = n < 8 ? a0 : a1;
#pragma unroll
      for (int k = 0; k < 4; ++k) v[k] += dppf<ROR8>(v[k]);
      if (n < 8) *(LAS f32x4*)(L + L_QP + ((w * 2 + (g >> 1)) * 64 + 8 * n + 4 * (g & 1)) * 4) = v;
      flag_set(L, F_QP + w, lane); }
    const u64 ep = __hip_atomic_load((const GAS u64*)(a.ws + WS_EPOCH), __ATOMIC_RELAXED, __HIP_MEMORY_SCOPE_AGENT) + 1ull;
    const unsigned elo = (unsigned)ep, ehi = (unsigned)(ep >> 32);
    __builtin_amdgcn_s_setprio(0);
    cnt_wait<2>(L, F_SENT, 2u);
    flag_wait<4, 2>(L, F_KV);
    __builtin_amdgcn_s_setprio(3);
    v4u kf[16];
    { const int k8 = lane & 7, hq = lane >> 3;
      const int qb = WS_QA + ((cb * 32 + 16 * hgA + hq) * 8 + w) * 384 + k8 * 16, mo = WS_QA + ((cb * 32 + 16 * hgA + n) * 8 + w) * 384 + 256;
      v4u v0, v1, v2, v3; unsigned spins = 0;
      for (;;) {
        asm volatile("" ::: "memory");
        v0 = LD128(qb); v1 = LD128(qb + 128); v2 = LD128(qb + 8 * 3072); v3 = LD128(qb + 8 * 3072 + 128);
        const v4u mt = LD128(mo);
        __builtin_amdgcn_sched_barrier(0);
        if (spins == 0) {
          cnt_add(L, F_GO, lane);
          const int f = ((n & 3) << 2) | (n >> 2);
          const int rowb = L_KV + (16 * w + n) * 1024;
#pragma unroll
          for (int s = 0; s < 16; ++s) kf[s] = *(const LAS v4u*)(L + rowb + (s >> 2) * 256 + (((4 * g + (s & 3)) ^ f) * 16)); }
        __builtin_amdgcn_sched_barrier(0);
        unsigned U = 0, V = 0; cs(v0, U, V); cs(v1, U, V); cs(v2, U, V); cs(v3, U, V);
        if (lane < 16) { U -= mt.x + mt.z - 2u * elo; V -= mt.y + mt.w - 2u * ehi; }
        if ((wsum(U) | wsum(V)) == 0u || ++spins > SPIN_CAP) break;
        __builtin_amdgcn_s_sleep(6);
      }
      auto add2 = [](v4u p, v4u q) { v4u o;
        o.x = pk2(bflo(p.x) + bflo(q.x), bfhi(p.x) + bfhi(q.x)); o.y = pk2(bflo(p.y) + bflo(q.y), bfhi(p.y) + bfhi(q.y));
        o.z = pk2(bflo(p.z) + bflo(q.z), bfhi(p.z) + bfhi(q.z)); o.w = pk2(bflo(p.w) + bflo(q.w), bfhi(p.w) + bfhi(q.w)); return o; };
      const int wb = L_QA + (4 * (w >> 1) + (k8 & 3)) * 1024 + (2 * (w & 1) + (k8 >> 2)) * 256, rot = hq + 4 * (k8 & 3) + 2 * (w & 1);
      *(LAS v4u*)(L + wb + (rot & 15) * 16) = add2(v0, v1);
      *(LAS v4u*)(L + wb + ((rot + 8) & 15) * 16) = add2(v2, v3);
      flag_set(L, F_QA + w, lane);
    }
    STAMP(2);
    flag_wait<8, SPN>(L, F_QA);
    STAMP(3);
    f32x4 sc = z4, sc2 = z4;
#pragma unroll
    for (int hs = 0; hs < 2; ++hs) {
      v4u qf[8];
#pragma unroll
      for (int s = 0; s < 8; ++s) qf[s] = *(const LAS v4u*)(L + L_QA + (8 * hs + s) * 1024 + g * 256 + ((n + 4 * (s & 3) + 2 * (g >> 1)) & 15) * 16);
      __builtin_amdgcn_sched_barrier(0);
#pragma unroll
      for (int s = 0; s < 8; s += 2) { sc = mfma(kf[8 * hs + s], qf[s], sc); sc2 = mfma(kf[8 * hs + s + 1], qf[s + 1], sc2); }
    }
    sc += sc2;
    { float m = fmaxf(fmaxf(sc.x, sc.y), fmaxf(sc.z, sc.w)); m = x32maxf(x16maxf(m));
      if (g == 0) *(volatile LAS float*)(L + L_SM + n * 32 + w * 4) = m;
      flag_set(L, F_SM + w, lane); }
    float mcu;
    { v4u m0, m1;
      flag_wait<8, SPN>(L, F_SM); lds_rd2((unsigned)(size_t)L + L_SM + n * 32, m0, m1);
      STAMP(9);
      const f32x4 f0 = __builtin_bit_cast(f32x4, m0), f1 = __builtin_bit_cast(f32x4, m1);
      mcu = fmaxf(fmaxf(fmaxf(f0.x, f0.y), fmaxf(f0.z, f0.w)), fmaxf(fmaxf(f1.x, f1.y), fmaxf(f1.z, f1.w)));
      const unsigned p01 = pk2(__builtin_amdgcn_exp2f(sc.x - mcu), __builtin_amdgcn_exp2f(sc.y - mcu));
      const unsigned p23 = pk2(__builtin_amdgcn_exp2f(sc.z - mcu), __builtin_amdgcn_exp2f(sc.w - mcu));
      *(volatile LAS v2u*)(L + L_P + (w >> 1) * 1024 + (n + 16 * (2 * (w & 1) + (g >> 1))) * 16 + 8 * (g & 1)) = v2u{p01, p23};
      float l = (bflo(p01) + bfhi(p01)) + (bflo(p23) + bfhi(p23)); l = x32addf(x16addf(l));
      if (g == 0) *(volatile LAS float*)(L + L_SS + n * 32 + w * 4) = l;
      flag_set(L, F_SS + w, lane); }
    { const int q4 = n >> 2, p4 = n & 3;
      const unsigned base = L_KV + (8 * g + q4) * 1024 + (w >> 1) * 256 + ((((8 * (w & 1)) + (p4 >> 1)) ^ ((q4 << 2) | (2 * (g & 1)))) * 16) + 8 * (p4 & 1);
      bf16x8 vf[16];
#pragma unroll
      for (int ks = 0; ks < 4; ++ks)
#pragma unroll
        for (int T = 0; T < 4; ++T) {
          const s16x4 lo = __builtin_amdgcn_ds_read_tr16_b64_v4i16((LAS s16x4*)(L + ((base ^ (unsigned)(T << 5)) + ks * 32768)));
          const s16x4 hi = __builtin_amdgcn_ds_read_tr16_b64_v4i16((LAS s16x4*)(L + ((base ^ (unsigned)((T << 5) | (1 << 4) | (1 << 12))) + ks * 32768)));
          vf[ks * 4 + T] = __builtin_shufflevector(lo, hi, 0, 1, 2, 3, 4, 5, 6, 7); }
      __builtin_amdgcn_sched_barrier(0);
      v4u l0, l1;
      flag_wait<8, SPN>(L, F_SS);
      l0 = *(const LAS v4u*)(L + L_SS + n * 32); l1 = *(const LAS v4u*)(L + L_SS + n * 32 + 16);
      STAMP(10);
      f32x4 o[4] = {z4, z4, z4, z4};
      v4u pf[4];
#pragma unroll
      for (int ks = 0; ks < 4; ++ks) pf[ks] = *(const LAS v4u*)(L + L_P + ks * 1024 + lane * 16);
#pragma unroll
      for (int ks = 0; ks < 4; ++ks)
#pragma unroll
        for (int T = 0; T < 4; ++T) o[T] = __builtin_amdgcn_mfma_f32_16x16x32_bf16(vf[ks * 4 + T], __builtin_bit_cast(bf16x8, pf[ks]), o[T], 0, 0, 0);
      const f32x4 f0 = __builtin_bit_cast(f32x4, l0), f1 = __builtin_bit_cast(f32x4, l1);
      const float lcu = ((f0.x + f0.y) + (f0.z + f0.w)) + ((f1.x + f1.y) + (f1.z + f1.w));
      const int row = ((16 * hgA + n) * 4 + (w >> 1)) * 128 + kb;
      asm volatile("" :: "v"(o[3]));
      unsigned U = 0, V = 0;
#pragma unroll
      for (int T = 0; T < 4; T += 2) {
        unsigned a0 = pk2(o[T].x, o[T].y), a1 = pk2(o[T].z, o[T].w), b0 = pk2(o[T + 1].x, o[T + 1].y), b1 = pk2(o[T + 1].z, o[T + 1].w);
        auto s0 = __builtin_amdgcn_permlane16_swap(a0, b0, false, false); auto s1 = __builtin_amdgcn_permlane16_swap(a1, b1, false, false);
        const v4u ov = {s0[0], s1[0], s0[1], s1[1]};
        ST128(ov, WS_PO + row * 256 + (8 * (w & 1) + 2 * (T + (g & 1)) + (g >> 1)) * 16);
        cs(ov, U, V);
      }
      U = x32addu(x16addu(U)); V = x32addu(x16addu(V));
      const unsigned mb = __float_as_uint(mcu), lb = __float_as_uint(lcu);
      if (g < 2) ST128((g == 0 ? v4u{mb, lb, d2(mb, 0x2B5D71C3u, d2(lb, 0x8F63E917u, U + elo)), d2(mb, 0xD1A34E8Bu, d2(lb, 0x47F92C6Du, V + ehi))} : v4u{elo, ehi, elo, ehi}), WS_PM + (row * 2 + (w & 1)) * 32 + g * 16); }
    cnt_add(L, F_PUB, lane);
    STAMP(4);
    v4u uv[2];
#pragma unroll
    for (int t = 0; t < 2; ++t) uv[t] = ldnt(a.wuv + ((size_t)(128 * cq + 16 * w + 8 * t + 2 * g + (n >> 3)) * 32 + h) * 128 + 64 * dh + 8 * (n & 7));
    __builtin_amdgcn_sched_barrier(0);
    { const int row0 = (h * 4 + cq) * 128 + 8 * w + g;
      v4u p[4], mt[4]; unsigned spins = 0;
      __builtin_amdgcn_s_sleep(SLH2);
      for (;;) {
        asm volatile("" ::: "memory");
#pragma unroll
        for (int t = 0; t < 4; ++t) { const int row = row0 + 4 * (t & 1) + 64 * (t >> 1); p[t] = LD128(WS_PO + row * 256 + n * 16); mt[t] = LD128(WS_PM + (row * 2 + (n >> 3)) * 32); }
        __builtin_amdgcn_sched_barrier(0);
        if (spins == 0) cnt_add(L, F_H2, lane);
        __builtin_amdgcn_sched_barrier(0);
        unsigned U = 0, V = 0, Um = 0, Vm = 0;
#pragma unroll
        for (int t = 0; t < 4; ++t) { cs(p[t], U, V);
          Um = d2(mt[t].x, 0x2B5D71C3u, d2(mt[t].y, 0x8F63E917u, Um - mt[t].z)); Vm = d2(mt[t].x, 0xD1A34E8Bu, d2(mt[t].y, 0x47F92C6Du, Vm - mt[t].w)); }
        if ((n & 7) == 0) { U += Um + 4u * elo; V += Vm + 4u * ehi; }
        if ((wsum(U) | wsum(V)) == 0u || ++spins > SPIN_CAP) break;
        const int to = WS_PM + ((((h * 4 + cq) * 128 + 64 * (lane >> 5) + 8 * w) * 2) * 32) + (lane & 31) * 16;
        for (;;) {
          __builtin_amdgcn_s_sleep(SLT);
          asm volatile("" ::: "memory");
          const v2u tg = LD64(to);
          if (__all((lane & 1) == 0 || (tg.x == elo && tg.y == ehi)) || ++spins > SPIN_CAP) break;
        }
      }
      STAMP(5);
      f32x4 acc = z4; float Mb[2], Lb[2];
#pragma unroll
      for (int b = 0; b < 2; ++b) {
        const float m0 = __uint_as_float(mt[2 * b].x), m1 = __uint_as_float(mt[2 * b + 1].x);
        Mb[b] = x32maxf(x16maxf(fmaxf(m0, m1)));
        const unsigned wp = pk2(__builtin_amdgcn_exp2f(m0 - Mb[b]), __builtin_amdgcn_exp2f(m1 - Mb[b]));
        const bool mine = (n >> 3) == b;
        acc = mfma(diag(dg, mine ? (wp & 0xffffu) : 0u), p[2 * b], acc); acc = mfma(diag(dg, mine ? (wp >> 16) : 0u), p[2 * b + 1], acc);
        Lb[b] = x32addf(x16addf(bflo(wp) * __uint_as_float(mt[2 * b].y) + bfhi(wp) * __uint_as_float(mt[2 * b + 1].y)));
      }
      *(volatile LAS f32x4*)(L + L_CP + ((w * 2 + (g >> 1)) * 128 + 8 * n + 4 * (g & 1)) * 4) = acc;
      if (lane < 2) *(volatile LAS v2u*)(L + L_CS + (lane * 8 + w) * 8) = v2u{__float_as_uint(lane ? Mb[1] : Mb[0]), __float_as_uint(lane ? Lb[1] : Lb[0])};
      flag_set(L, F_CS + w, lane); }
    STAMP(1);
    { const int b = n >> 3; float fk[8];
      { v4u s[4]; unsigned gv;
        do { gv = *(volatile LAS unsigned*)(L + L_G + b * 4); } while (!__all(gv != SENT));
        flag_wait<8, SPN>(L, F_CS);
#pragma unroll
        for (int k = 0; k < 4; ++k) s[k] = *(const LAS v4u*)(L + L_CS + b * 64 + k * 16);
        f32x2 c[16];
        __builtin_amdgcn_sched_barrier(0);
        lds_rd16x2((unsigned)(size_t)L + L_CP + (b * 128 + 16 * w + 2 * g) * 4, c);
        STAMP(8);
        float M = -INFINITY, Ls = 0.f;
#pragma unroll
        for (int k = 0; k < 4; ++k) M = fmaxf(M, fmaxf(__uint_as_float(s[k].x), __uint_as_float(s[k].z)));
#pragma unroll
        for (int k = 0; k < 4; ++k) { fk[2 * k] = __builtin_amdgcn_exp2f(__uint_as_float(s[k].x) - M); fk[2 * k + 1] = __builtin_amdgcn_exp2f(__uint_as_float(s[k].z) - M);
          Ls = fmaf(fk[2 * k], __uint_as_float(s[k].y), fmaf(fk[2 * k + 1], __uint_as_float(s[k].w), Ls)); }
        const float sg = __uint_as_float(gv) * __builtin_amdgcn_rcpf(Ls);
        f32x4 a0 = z4, a1 = z4;
#pragma unroll
        for (int t = 0; t < 2; ++t) { float s0 = 0.f, s1 = 0.f;
#pragma unroll
          for (int k = 0; k < 8; ++k) { s0 = fmaf(fk[k], c[t * 8 + k].x, s0); s1 = fmaf(fk[k], c[t * 8 + k].y, s1); }
          const unsigned sp = pk2(s0 * sg, s1 * sg);
          a0 = mfma(diag(dg, sp & 0xffffu), uv[t], a0); a1 = mfma(diag(dg, sp >> 16), uv[t], a1); }
        f32x4 v = n < 8 ? a0 : a1;
#pragma unroll
        for (int k = 0; k < 4; ++k) v[k] += dppf<ROR8>(v[k]);
        if (n < 8) *(volatile LAS f32x4*)(L + L_XP + ((w * 2 + (g >> 1)) * 64 + 8 * n + 4 * (g & 1)) * 4) = v; } }
    flag_set(L, F_XP + w, lane);
    STAMP(13);
    if (w < 2) {
      flag_wait<8, SPN>(L, F_XP);
      STAMP(14);
      float s = 0.f;
#pragma unroll
      for (int k = 0; k < 8; ++k) s += *(const LAS float*)(L + L_XP + ((k * 2 + w) * 64 + lane) * 4);
      *(volatile LAS unsigned*)(L + L_CTX + (w * 64 + (lane & 3) * 16 + (((lane >> 2) - 4 * cq) & 15)) * 4) = __float_as_uint(s);
      flag_set(L, F_CTX + w, lane);
    }
    STAMP(6);
    if (w >= 2) {
      const int t6 = tid - 128, f = t6 >> 2, pt = t6 & 3, off = ((f / 48) * MD + 12 * (f % 48) + 4 * (pt < 3 ? pt : 0)) * 4;
      __builtin_amdgcn_s_setprio(0);
      flag_wait<7, 1>(L, F_OUT);
      __builtin_amdgcn_s_setprio(3);
      STAMP(14);
      const f32x4 s = *(const LAS f32x4*)(L + L_OT + off) + *(const LAS f32x4*)(L + L_OF + off);
      v4u o = __builtin_bit_cast(v4u, s);
      unsigned U = 0, V = 0; cs(o, U, V); if (pt == 3) { U = elo; V = ehi; }
      U += dppu<XOR1>(U); U += dppu<XOR2>(U); V += dppu<XOR1>(V); V += dppu<XOR2>(V);
      if (pt == 3) o = v4u{U, V, 0u, 0u};
      ST128(o, WS_FO + f * 16384 + wg * 64 + pt * 16);
    }
    STAMP(7);
    if (wg < 96) {
      const int pt = lane & 3;
      v4u v0, v1; unsigned spins = 0;
      if (w < 2) flag_wait<7, 2>(L, F_OUT);
      __builtin_amdgcn_s_sleep(SLH3);
      for (;;) {
        asm volatile("" ::: "memory");
        v0 = LD128(WS_FO + wg * 16384 + tid * 16); v1 = LD128(WS_FO + wg * 16384 + 8192 + tid * 16);
        unsigned U = 0, V = 0; cs(v0, U, V); cs(v1, U, V);
        if (pt == 3) { U = 2u * elo - v0.x - v1.x; V = 2u * ehi - v0.y - v1.y; }
        if ((wsum(U) | wsum(V)) == 0u || ++spins > SPIN_CAP) break;
        __builtin_amdgcn_s_sleep(4);
      }
      f32x4 s = __builtin_bit_cast(f32x4, v0) + __builtin_bit_cast(f32x4, v1);
#pragma unroll
      for (int k = 0; k < 4; ++k) { float t = s[k]; t += dppf<ROR4>(t); t += dppf<ROR8>(t); t = x32addf(x16addf(t)); s[k] = t; }
      if (lane < 3) *(volatile LAS f32x4*)(L + L_FIN + w * 64 + lane * 16) = s;
      flag_set(L, F_FIN + w, lane);
      STAMP(11);
      if (w == 0) {
        const int b = wg / 48, col = 12 * (wg % 48) + (lane < 12 ? lane : 0);
        const float bb = (1.f - *(const LAS float*)(L + L_G + b * 4)) * bf1(a.b2[col]);
        flag_wait<8, SPN>(L, F_FIN);
        if (lane < 12) {
          float t = bb;
#pragma unroll
          for (int k = 0; k < 8; ++k) t += *(const LAS float*)(L + L_FIN + k * 64 + lane * 4);
          a.out[b * MD + col] = (u16)bfr(t);
          if (wg == 0 && lane == 0) __hip_atomic_store((GAS u64*)(a.ws + WS_EPOCH), ep, __ATOMIC_RELAXED, __HIP_MEMORY_SCOPE_AGENT);
        }
        if (wg == 0) {
          const unsigned te = (unsigned)__builtin_amdgcn_s_memrealtime();
          unsigned t[4];
#pragma unroll
          for (int k = 0; k < 4; ++k) t[k] = __hip_atomic_load((GAS unsigned*)(a.ws + WS_STAMP + (lane + 64 * k) * 4), __ATOMIC_RELAXED, __HIP_MEMORY_SCOPE_AGENT);
          unsigned amax = 0, amin = 0xffffffffu;
#pragma unroll
          for (int k = 0; k < 4; ++k) { const unsigned ag = te - t[k]; amax = max(amax, ag); amin = min(amin, ag); }
          for (int o = 32; o; o >>= 1) { amax = max(amax, (unsigned)__shfl_xor((int)amax, o)); amin = min(amin, (unsigned)__shfl_xor((int)amin, o)); }
          const unsigned own = te - (unsigned)__builtin_amdgcn_readfirstlane((int)t[0]);
          unsigned d = PROBE == 0 ? 0u : PROBE == 1 ? amax - amin : PROBE == 2 ? amax : PROBE == 3 ? amin : own;
          d = min(d, 4000u) * 10u;
          while ((unsigned)__builtin_amdgcn_s_memrealtime() - te < d) __builtin_amdgcn_s_sleep(2);
        }
      }
    }
  } else if (wave < 10) {
    const int kw = wave - 8;
    const u64 ep = __hip_atomic_load((const GAS u64*)(a.ws + WS_EPOCH), __ATOMIC_RELAXED, __HIP_MEMORY_SCOPE_AGENT) + 1ull;
    const unsigned elo = (unsigned)ep, ehi = (unsigned)(ep >> 32);
    { v4u uk[8];
#pragma unroll
      for (int rg = 0; rg < 8; ++rg) uk[rg] = ldnt(a.wuk + ((size_t)(128 * cq + 64 * kw + 8 * (n & 7) + rg) * 32 + h) * 128 + 64 * dh + 32 * (n >> 3) + 8 * g);
      __builtin_amdgcn_sched_barrier(0);
      bar();
      __builtin_amdgcn_s_setprio(3);
      STAMP(8);
      flag_wait<8, SPN>(L, F_QP);
      STAMP(10);
      { f32x2 s = {0.f, 0.f};
#pragma unroll
        for (int k = 0; k < 8; ++k) s += *(const LAS f32x2*)(L + L_QP + ((k * 2 + (lane >> 5)) * 64 + 2 * (lane & 31)) * 4);
        *(volatile LAS unsigned*)(L + L_QB + kw * 256 + lane * 4) = pk2(s.x, s.y); }
      v4u qa0 = lds_v4(L, L_QB + kw * 256 + (n & 1) * 128 + g * 16), qa1 = lds_v4(L, L_QB + kw * 256 + (n & 1) * 128 + 64 + g * 16);
      if (n >= 2) { qa0 = v4u{0u, 0u, 0u, 0u}; qa1 = qa0; }
      constexpr float SC = 0.08838834764831845f * 1.4426950408889634f;
      float sel[8];
#pragma unroll
      for (int rg = 0; rg < 8; ++rg) {
        const f32x4 a1 = mfma(qa0, uk[rg], z4), a2 = mfma(qa1, uk[rg], z4);
        float v0 = n < 8 ? a1.x : a2.x, v1 = n < 8 ? a1.y : a2.y;
        v0 += dppf<ROR8>(v0); v1 += dppf<ROR8>(v1);
        sel[rg] = (n < 8 ? v0 : v1) * SC;
      }
      const v4u o = {pk2(sel[0], sel[1]), pk2(sel[2], sel[3]), pk2(sel[4], sel[5]), pk2(sel[6], sel[7])};
      const int qb = WS_QA + (((n >> 3) * 32 + h) * 8 + 2 * cq + kw) * 384;
      if (lane < 16) ST128(o, qb + dh * 128 + (n & 7) * 16);
      unsigned U = 0, V = 0; cs(o, U, V);
      U += dppu<XOR1>(U); U += dppu<XOR2>(U); U += dppu<HMIR>(U); V += dppu<XOR1>(V); V += dppu<XOR2>(V); V += dppu<HMIR>(V);
      if (lane < 16 && (n & 7) == 0) ST64((v2u{U + elo, V + ehi}), qb + 256 + dh * 8);
      cnt_add(L, F_SENT, lane); }
    __builtin_amdgcn_s_setprio(0);
    STAMP(1);
    cnt_wait<4>(L, F_PUB, 8u);
    f32x4 acc[3] = {z4, z4, z4};
#pragma unroll
    for (int pass = 0; pass < 2; ++pass) {
      const int it = wg + 256 * pass;
      if (pass == 1 && wg >= 32) break;
      const int hb = L_HID + pass * 128;
      v4u w2v[12];
      const u16* wp = a.w2 + (size_t)(16 * it + g) * MD + 384 * kw;
#pragma unroll
      for (int t = 0; t < 4; ++t) { w2v[t] = ldnt(wp + (size_t)4 * t * MD + 8 * n); w2v[4 + t] = ldnt(wp + (size_t)4 * t * MD + 128 + 8 * (kw ? (n & 7) : n)); }
      if (kw == 0) {
#pragma unroll
        for (int t = 0; t < 4; ++t) w2v[8 + t] = ldnt(wp + (size_t)4 * t * MD + 256 + 8 * n);
        const v2u b1p = *(const v2u*)(a.b1 + 16 * it + (n & 1) * 8 + 4 * (g & 1));
        v4u v[6]; unsigned spins = 0;
        for (;;) {
          asm volatile("" ::: "memory");
#pragma unroll
          for (int k = 0; k < 6; ++k) v[k] = LD128(WS_HX + ((it >> 3) * 6 + k) * 1152 + lane * 16);
          const v2u mt = LD64(WS_HX + ((it >> 3) * 6 + (lane < 6 ? lane : 0)) * 1152 + 1024);
          unsigned U = 0, V = 0;
#pragma unroll
          for (int k = 0; k < 6; ++k) cs(v[k], U, V);
          if (lane < 6) { U -= mt.x - elo; V -= mt.y - ehi; }
          if ((wsum(U) | wsum(V)) == 0u || ++spins > SPIN_CAP) break;
          __builtin_amdgcn_s_sleep(20);
        }
        unsigned gv; do { gv = *(volatile LAS unsigned*)(L + L_G + (g >> 1) * 4); } while (!__all(gv != SENT));
        f32x4 s = z4;
#pragma unroll
        for (int k = 0; k < 6; ++k) s += __builtin_bit_cast(f32x4, v[k]);
        if ((n >> 1) == (it & 7)) {
          const float og = 1.f - __uint_as_float(gv);
          f32x4 hv; hv.x = fmaxf(s.x + bflo(b1p.x), 0.f); hv.y = fmaxf(s.y + bfhi(b1p.x), 0.f); hv.z = fmaxf(s.z + bflo(b1p.y), 0.f); hv.w = fmaxf(s.w + bfhi(b1p.y), 0.f);
          *(volatile LAS f32x4*)(L + hb + ((g >> 1) * 16 + 8 * (n & 1) + 4 * (g & 1)) * 4) = hv * hv * og;
        }
        cnt_add(L, F_HID, lane);
      } else cnt_wait<4>(L, F_HID, (unsigned)pass + 1u);
#pragma unroll
      for (int t = 0; t < 4; ++t) { const v4u d = diag(dg, bfr(*(volatile LAS float*)(L + hb + ((n >> 3) * 16 + 4 * t + g) * 4)));
        acc[0] = mfma(d, w2v[t], acc[0]); acc[1] = mfma(d, w2v[4 + t], acc[1]); if (kw == 0) acc[2] = mfma(d, w2v[8 + t], acc[2]); }
    }
    { const int ob = L_OF + ((g >> 1) * MD + 384 * kw + 8 * n + 4 * (g & 1)) * 4;
      *(volatile LAS f32x4*)(L + ob) = acc[0];
      if (kw == 0 || n < 8) *(volatile LAS f32x4*)(L + ob + 512) = acc[1];
      if (kw == 0) *(volatile LAS f32x4*)(L + ob + 1024) = acc[2]; }
    flag_set(L, F_OUT + (5 + kw), lane);
    STAMP(3);
  } else if (wave < 15) {
    const int T = wave - 10;
    if (wave == 14) {
      const unsigned char* cp = (const unsigned char*)(pc0 & ~127ull) + (r * 8 + (lane & 7)) * 128;
      if (lane < 8 && (r * 8 + lane) * 128 < CODEPF) { const unsigned v = *(const volatile GAS unsigned*)cp; asm volatile("" :: "v"(v)); }
    } else if (wave >= 12) { const int t2 = tid - 768; if (t2 < (L_S1 - L_S0) / 16) *(LAS v4u*)(L + L_S0 + t2 * 16) = v4u{SENT, SENT, SENT, SENT}; }
    bar();
    const u16* p = a.wo + (size_t)(h * 128 + 64 * dh) * MD;
    if (T < 4) {
      { const unsigned char* src = (const unsigned char*)(a.ckv + (size_t)kb * 128 * 512);
#pragma unroll 1
        for (int i = T; i < 128; i += 4) {
          const int row = (i + 64 * hgA) & 127;
          const int f = ((row & 3) << 2) | ((row >> 2) & 3);
          glds16_nt(src + row * 1024 + ((lane ^ f) * 16), (unsigned)(size_t)(L + L_KV) + row * 1024);
          if (i < 4 * NTH) asm volatile("s_waitcnt vmcnt(%0)" :: "n"(WKV - 1) : "memory");
        } }
      cnt_add(L, F_DMAI, lane);
      STAMP(8);
      VMWAIT(0);
      flag_set(L, F_KV + T, lane);
      STAMP(1);
      cnt_wait<2>(L, F_GO, 8u);
      v4u wo[16];
#pragma unroll
      for (int t = 0; t < 16; ++t) {
        if (t == ST1) { __builtin_amdgcn_sched_barrier(0); cnt_wait<3>(L, F_PUB, 8u); }
        if (t == ST2) { __builtin_amdgcn_sched_barrier(0); cnt_wait<3>(L, F_H2, 8u); }
        wo[t] = ldnt(p + (size_t)(4 * ((t + 4 * cq) & 15) + g) * MD + 128 * T + 8 * n); }
      __builtin_amdgcn_sched_barrier(0);
      STAMP(2);
      flag_wait<1, 4>(L, F_CS);
      __builtin_amdgcn_s_setprio(3);
      f32x4 acc = z4; v4u c[4];
      flag_wait<2, SPN>(L, F_CTX);
      lds_rd4((unsigned)(size_t)L + L_CTX + ((n >> 3) * 64 + g * 16) * 4, c[0], c[1], c[2], c[3]);
      STAMP(9);
#pragma unroll
      for (int t4 = 0; t4 < 4; ++t4) { const unsigned c01 = pk2(__uint_as_float(c[t4].x), __uint_as_float(c[t4].y)), c23 = pk2(__uint_as_float(c[t4].z), __uint_as_float(c[t4].w));
        acc = mfma(diag(dg, c01 & 0xffffu), wo[4 * t4], acc); acc = mfma(diag(dg, c01 >> 16), wo[4 * t4 + 1], acc);
        acc = mfma(diag(dg, c23 & 0xffffu), wo[4 * t4 + 2], acc); acc = mfma(diag(dg, c23 >> 16), wo[4 * t4 + 3], acc); }
      *(volatile LAS f32x4*)(L + L_OT + ((g >> 1) * MD + 128 * T + 8 * n + 4 * (g & 1)) * 4) = acc;
    } else {
      cnt_wait<4>(L, F_PUB, 8u);
      v4u wo[8];
#pragma unroll
      for (int t = 0; t < 8; ++t) wo[t] = ldnt(p + (size_t)(8 * ((t + 2 * cq) & 7) + 2 * g + (n >> 3)) * MD + 512 + 8 * (n & 7));
      __builtin_amdgcn_sched_barrier(0);
      flag_wait<1, 4>(L, F_CS);
      __builtin_amdgcn_s_setprio(3);
      v4u c[8];
      flag_wait<2, SPN>(L, F_CTX);
      lds_rd8((unsigned)(size_t)L + L_CTX + ((n >> 3) * 64 + (2 * (g & 1)) * 16) * 4, c);
      f32x4 a0 = z4, a1 = z4;
#pragma unroll
      for (int t = 0; t < 8; ++t) { const int e = 2 * (t & 1);
        const unsigned u0 = (g >> 1) ? c[t >> 1][e + 1] : c[t >> 1][e], u1 = (g >> 1) ? c[4 + (t >> 1)][e + 1] : c[4 + (t >> 1)][e];
        const unsigned cp = pk2(__uint_as_float(u0), __uint_as_float(u1));
        a0 = mfma(diag(dg, cp & 0xffffu), wo[t], a0); a1 = mfma(diag(dg, cp >> 16), wo[t], a1); }
      f32x4 v = n < 8 ? a0 : a1;
#pragma unroll
      for (int k = 0; k < 4; ++k) v[k] += dppf<ROR8>(v[k]);
      if (n < 8) *(volatile LAS f32x4*)(L + L_OT + ((g >> 1) * MD + 512 + 8 * n + 4 * (g & 1)) * 4) = v;
    }
    flag_set(L, F_OUT + T, lane);
    STAMP(3);
  } else {
    bar();
    cnt_wait<2>(L, F_X, 1u);
    const u64 ep = __hip_atomic_load((const GAS u64*)(a.ws + WS_EPOCH), __ATOMIC_RELAXED, __HIP_MEMORY_SCOPE_AGENT) + 1ull;
    const unsigned elo = (unsigned)ep, ehi = (unsigned)(ep >> 32);
    { float s0 = 0.f, s1 = 0.f;
      if (lane < 36) {
        const v4u gw = *(const v4u*)(a.gw + 8 * lane), gw2 = *(const v4u*)(a.gw + 288 + 8 * lane);
        const v4u xa = *(const LAS v4u*)(L + L_X + 16 * lane), xa2 = *(const LAS v4u*)(L + L_X + 576 + 16 * lane), xb = *(const LAS v4u*)(L + L_X + 1152 + 16 * lane), xb2 = *(const LAS v4u*)(L + L_X + 1728 + 16 * lane);
#pragma unroll
        for (int k = 0; k < 4; ++k) {
          s0 += bflo(gw[k]) * bflo(xa[k]) + bfhi(gw[k]) * bfhi(xa[k]) + bflo(gw2[k]) * bflo(xa2[k]) + bfhi(gw2[k]) * bfhi(xa2[k]);
          s1 += bflo(gw[k]) * bflo(xb[k]) + bfhi(gw[k]) * bfhi(xb[k]) + bflo(gw2[k]) * bflo(xb2[k]) + bfhi(gw2[k]) * bfhi(xb2[k]); }
      }
      s0 = wsumf(s0); s1 = wsumf(s1);
      if (lane < 2) *(volatile LAS unsigned*)(L + L_G + lane * 4) = min(__float_as_uint(1.f / (1.f + __expf(-(lane ? s1 : s0)))), SENT - 1u); }
    if (wg < 216) {
      const int T = wg / 6, rg = wg % 6;
      cnt_wait<4>(L, F_DMAI, 4u);
      const u16* p = a.w1 + (size_t)(96 * rg + 24 * g) * NF + 128 * T + 8 * n;
      v4u wv[24];
#pragma unroll
      for (int t = 0; t < 24; ++t) wv[t] = ldnt(p + (size_t)t * NF);
      __builtin_amdgcn_sched_barrier(0);
      v4u xs[3];
#pragma unroll
      for (int k = 0; k < 3; ++k) xs[k] = *(const LAS v4u*)(L + L_X + (n >> 3) * 1152 + (96 * rg + 24 * g) * 2 + 16 * k);
      f32x4 acc = z4;
#pragma unroll
      for (int t = 0; t < 24; ++t) { const unsigned xd = xs[t >> 3][(t >> 1) & 3]; acc = mfma(diag(dg, (t & 1) ? (xd >> 16) : (xd & 0xffffu)), wv[t], acc); }
      const v4u o = __builtin_bit_cast(v4u, acc);
      ST128(o, WS_HX + wg * 1152 + lane * 16);
      unsigned U = 0, V = 0; cs(o, U, V); U = wsum(U); V = wsum(V);
      if (lane == 0) ST64((v2u{U + elo, V + ehi}), WS_HX + wg * 1152 + 1024);
      STAMP(3);
    }
  }
  STAMP_OUT();
}
}

extern "C" void kernel_launch(void* const* d_in, const int* in_sizes, int n_in, void* d_out, int out_size, void* d_ws, size_t ws_size, hipStream_t stream) {
  Args a;
  a.x = (const u16*)d_in[0]; a.ckv = (const u16*)d_in[1]; a.wq = (const u16*)d_in[2]; a.wuk = (const u16*)d_in[3]; a.wuv = (const u16*)d_in[4]; a.wo = (const u16*)d_in[5];
  a.w1 = (const u16*)d_in[7]; a.b1 = (const u16*)d_in[8]; a.w2 = (const u16*)d_in[9]; a.b2 = (const u16*)d_in[10]; a.gw = (const u16*)d_in[11];
  a.out = (u16*)d_out; a.ws = (unsigned char*)d_ws;
  (void)hipFuncSetAttribute((const void*)fused_kernel, hipFuncAttributeMaxDynamicSharedMemorySize, L_END);
  hipLaunchKernelGGL(fused_kernel, dim3(256), dim3(1024), L_END, stream, a);
}
```

```cpp
#include <hip/hip_runtime.h>
#include <hip/hip_bf16.h>
#include <stdint.h>

typedef unsigned short u16;
typedef unsigned long long u64;
typedef unsigned v4u __attribute__((ext_vector_type(4)));
typedef unsigned v2u __attribute__((ext_vector_type(2)));
typedef float f32x4 __attribute__((ext_vector_type(4)));
typedef float f32x2 __attribute__((ext_vector_type(2)));
typedef short bf16x8 __attribute__((ext_vector_type(8)));
typedef short s16x4 __attribute__((ext_vector_type(4)));
#define LAS __attribute__((address_space(3)))
#define GAS __attribute__((address_space(1)))
#define DI __device__ __forceinline__

namespace {
constexpr int MD = 576, NF = 4608;
constexpr int WS_EPOCH = 0;
constexpr int WS_QA = 4096;
constexpr int WS_HX = WS_QA + 512 * 384;
constexpr int WS_PM = WS_HX + 216 * 1152;
constexpr int WS_PO = WS_PM + 128 * 4096;
constexpr int WS_FO = WS_PO + 128 * 32768;
constexpr int WS_END = WS_FO + 96 * 16384;
constexpr int L_KV = 0;
constexpr int L_QA = 131072;
constexpr int L_CP = L_QA, L_XP = L_QA + 8192;
constexpr int L_P = L_QA + 16384;
constexpr int L_QP = L_P;
constexpr int L_OF2 = L_KV + 8192;
constexpr int L_OT = L_KV;
constexpr int L_OF = L_P + 4096;
constexpr int L_X = L_OF + 4608;
constexpr int L_QB = L_X + 3072;
constexpr int L_HID = L_QB + 512;
constexpr int L_S0 = L_HID + 256;
constexpr int L_SM = L_S0;
constexpr int L_SS = L_SM + 512;
constexpr int L_CS = L_SS + 512;
constexpr int L_CTX = L_CS + 128;
constexpr int L_G = L_CTX + 512;
constexpr int L_FL = L_G + 16;
constexpr int F_QP = L_FL, F_QA = L_FL + 8, F_KV = L_FL + 16, F_XP = L_FL + 24, F_OUT = L_FL + 32, F_FIN = L_FL + 40, F_SM = L_FL + 48, F_SS = L_FL + 56, F_CS = L_FL + 64, F_CTX = L_FL + 72, F_MISC = L_FL + 192;
constexpr int F_X = F_MISC, F_ISS = F_MISC + 4, F_SENT = F_MISC + 8, F_DMAI = F_MISC + 12, F_HID = F_MISC + 16, F_GO = F_MISC + 20, F_PUB = F_MISC + 24, F_H2 = F_MISC + 28;
constexpr int L_S1 = L_FL + 224;
constexpr int L_FIN = L_S1;
constexpr int L_END = L_FIN + 512;
static_assert(L_END <= 163840 && (L_S1 - L_S0) % 16 == 0 && (L_S1 - L_S0) / 16 <= 128, "lds");
constexpr int SPN = 0, SLT = 8, SLH1 = 0, SLH2 = 24, SLH3 = 16, ST1 = 12, ST2 = 16, WKV = 5, NTH = 8;
constexpr int CODEPF = 30000;
constexpr unsigned SPIN_CAP = 1u << 15, SENT = 0xFFFFFFFFu;
constexpr int AUX_SC1 = 16;
constexpr u64 EPOCH_STEP = 0x9E3779B97F4A7C15ull;

struct Args {
  const u16 *x, *ckv, *wq, *wuk, *wuv, *wo, *w1, *b1, *w2, *b2, *gw;
  u16* out;
  unsigned char* ws;
};

DI void bar() { asm volatile("s_waitcnt lgkmcnt(0)" ::: "memory"); __builtin_amdgcn_s_barrier(); asm volatile("" ::: "memory"); }
DI v4u ldw(const void* p) { return *(const GAS v4u*)p; }
DI f32x4 mfma(v4u a, v4u b, f32x4 c) { return __builtin_amdgcn_mfma_f32_16x16x32_bf16(__builtin_bit_cast(bf16x8, a), __builtin_bit_cast(bf16x8, b), c, 0, 0, 0); }
DI float bflo(unsigned u) { return __uint_as_float(u << 16); }
DI float bfhi(unsigned u) { return __uint_as_float(u & 0xffff0000u); }
DI float bf1(u16 v) { return __uint_as_float(((unsigned)v) << 16); }
DI unsigned pk2(float lo, float hi) {
  typedef __bf16 bf2 __attribute__((ext_vector_type(2)));
  bf2 r; r.x = (__bf16)lo; r.y = (__bf16)hi; return __builtin_bit_cast(unsigned, r);
}
template <int CTRL> DI unsigned dppu(unsigned x) { return (unsigned)__builtin_amdgcn_mov_dpp((int)x, CTRL, 0xf, 0xf, true); }
template <int CTRL> DI float dppf(float x) { return __uint_as_float(dppu<CTRL>(__float_as_uint(x))); }
constexpr int XOR1 = 0xB1, XOR2 = 0x4E, HMIR = 0x141, ROR4 = 0x124, ROR8 = 0x128;
DI unsigned x16addu(unsigned x) { auto s = __builtin_amdgcn_permlane16_swap(x, x, false, false); return s[0] + s[1]; }
DI unsigned x32addu(unsigned x) { auto s = __builtin_amdgcn_permlane32_swap(x, x, false, false); return s[0] + s[1]; }
DI float x16addf(float x) { auto s = __builtin_amdgcn_permlane16_swap(__float_as_uint(x), __float_as_uint(x), false, false); return __uint_as_float(s[0]) + __uint_as_float(s[1]); }
DI float x32addf(float x) { auto s = __builtin_amdgcn_permlane32_swap(__float_as_uint(x), __float_as_uint(x), false, false); return __uint_as_float(s[0]) + __uint_as_float(s[1]); }
DI float x16maxf(float x) { auto s = __builtin_amdgcn_permlane16_swap(__float_as_uint(x), __float_as_uint(x), false, false); return fmaxf(__uint_as_float(s[0]), __uint_as_float(s[1])); }
DI float x32maxf(float x) { auto s = __builtin_amdgcn_permlane32_swap(__float_as_uint(x), __float_as_uint(x), false, false); return fmaxf(__uint_as_float(s[0]), __uint_as_float(s[1])); }
DI unsigned wsum(unsigned x) { x += dppu<XOR1>(x); x += dppu<XOR2>(x); x += dppu<ROR4>(x); x += dppu<ROR8>(x); return x32addu(x16addu(x)); }
DI float wsumf(float x) { x += dppf<XOR1>(x); x += dppf<XOR2>(x); x += dppf<ROR4>(x); x += dppf<ROR8>(x); return x32addf(x16addf(x)); }
DI unsigned d2(unsigned a, unsigned k, unsigned c) { typedef unsigned short us2 __attribute__((ext_vector_type(2)));
  return __builtin_amdgcn_udot2(__builtin_bit_cast(us2, a), __builtin_bit_cast(us2, k), c, false); }
DI void cs(v4u a, unsigned& U, unsigned& V) {
  U = d2(a.x, 0x9E37B5C5u, U); U = d2(a.y, 0x7A4DC2B3u, U); U = d2(a.z, 0xE4F1594Du, U); U = d2(a.w, 0x3B8FD16Fu, U);
  V = d2(a.x, 0x62E9F2A7u, V); V = d2(a.y, 0xC96B4F1Du, V); V = d2(a.z, 0x5573A9E5u, V); V = d2(a.w, 0xED2F8799u, V);
}
DI v4u lds_v4(LAS unsigned char* L, int off) { return *(volatile LAS v4u*)(L + off); }
DI void lds_rd2(unsigned ad, v4u& a, v4u& b) { asm volatile("ds_read_b128 %0, %2\n\tds_read_b128 %1, %2 offset:16\n\ts_waitcnt lgkmcnt(0)" : "=&v"(a), "=&v"(b) : "v"(ad) : "memory"); }
DI void lds_rd4(unsigned ad, v4u& a, v4u& b, v4u& c, v4u& d) {
  asm volatile("ds_read_b128 %0, %4\n\tds_read_b128 %1, %4 offset:16\n\tds_read_b128 %2, %4 offset:32\n\tds_read_b128 %3, %4 offset:48\n\ts_waitcnt lgkmcnt(0)" : "=&v"(a), "=&v"(b), "=&v"(c), "=&v"(d) : "v"(ad) : "memory"); }
DI void lds_rd8(unsigned ad, v4u (&c)[8]) {
  asm volatile("ds_read_b128 %0, %8\n\tds_read_b128 %1, %8 offset:16\n\tds_read_b128 %2, %8 offset:32\n\tds_read_b128 %3, %8 offset:48\n\t"
               "ds_read_b128 %4, %8 offset:64\n\tds_read_b128 %5, %8 offset:80\n\tds_read_b128 %6, %8 offset:96\n\tds_read_b128 %7, %8 offset:112\n\ts_waitcnt lgkmcnt(0)"
               : "=&v"(c[0]), "=&v"(c[1]), "=&v"(c[2]), "=&v"(c[3]), "=&v"(c[4]), "=&v"(c[5]), "=&v"(c[6]), "=&v"(c[7]) : "v"(ad) : "memory"); }
DI void lds_rd16x2(unsigned ad, f32x2 (&c)[16]) {
  asm volatile("ds_read_b64 %0, %16\n\tds_read_b64 %1, %16 offset:1024\n\tds_read_b64 %2, %16 offset:2048\n\tds_read_b64 %3, %16 offset:3072\n\t"
               "ds_read_b64 %4, %16 offset:4096\n\tds_read_b64 %5, %16 offset:5120\n\tds_read_b64 %6, %16 offset:6144\n\tds_read_b64 %7, %16 offset:7168\n\t"
               "ds_read_b64 %8, %16 offset:32\n\tds_read_b64 %9, %16 offset:1056\n\tds_read_b64 %10, %16 offset:2080\n\tds_read_b64 %11, %16 offset:3104\n\t"
               "ds_read_b64 %12, %16 offset:4128\n\tds_read_b64 %13, %16 offset:5152\n\tds_read_b64 %14, %16 offset:6176\n\tds_read_b64 %15, %16 offset:7200\n\ts_waitcnt lgkmcnt(0)"
               : "=&v"(c[0]), "=&v"(c[1]), "=&v"(c[2]), "=&v"(c[3]), "=&v"(c[4]), "=&v"(c[5]), "=&v"(c[6]), "=&v"(c[7]),
                 "=&v"(c[8]), "=&v"(c[9]), "=&v"(c[10]), "=&v"(c[11]), "=&v"(c[12]), "=&v"(c[13]), "=&v"(c[14]), "=&v"(c[15]) : "v"(ad) : "memory"); }
template <int N, int SLEEP> DI void flag_wait(LAS unsigned char* L, int off) {
  constexpr u64 MASK = N == 8 ? ~0ull : ((1ull << (8 * (N & 7))) - 1ull);
  while ((*(volatile LAS u64*)(L + off) & MASK) != 0ull) { if (SLEEP) __builtin_amdgcn_s_sleep(SLEEP); }
  asm volatile("" ::: "memory");
}
DI void flag_set(LAS unsigned char* L, int off, int lane) { asm volatile("" ::: "memory"); if (lane == 0) *(volatile LAS unsigned char*)(L + off) = 0; asm volatile("" ::: "memory"); }
template <int SLEEP> DI void cnt_wait(LAS unsigned char* L, int off, unsigned n) {
  while (*(volatile LAS unsigned*)(L + off) + 1u < n) { if (SLEEP) __builtin_amdgcn_s_sleep(SLEEP); }
  asm volatile("" ::: "memory");
}
DI void cnt_add(LAS unsigned char* L, int off, int lane) { asm volatile("" ::: "memory"); if (lane == 0) (void)__hip_atomic_fetch_add((LAS unsigned*)(L + off), 1u, __ATOMIC_RELAXED, __HIP_MEMORY_SCOPE_WORKGROUP); asm volatile("" ::: "memory"); }
struct Diag { unsigned m0, m1, m2, m3, sh; };
DI Diag make_diag(int i) { const int p = i & 7; Diag d; d.sh = (p & 1) * 16; d.m0 = (p >> 1) == 0 ? ~0u : 0u; d.m1 = (p >> 1) == 1 ? ~0u : 0u; d.m2 = (p >> 1) == 2 ? ~0u : 0u; d.m3 = (p >> 1) == 3 ? ~0u : 0u; return d; }
DI v4u diag(const Diag& d, unsigned vb16) { const unsigned v = vb16 << d.sh; return v4u{v & d.m0, v & d.m1, v & d.m2, v & d.m3}; }
DI unsigned bfr(float f) { return pk2(f, 0.f) & 0xffffu; }
DI void glds16(const void* gsrc, unsigned lds_dst) {
  unsigned keep;
  asm volatile("s_mov_b32 %0, m0\n\ts_mov_b32 m0, %2\n\ts_nop 0\n\tglobal_load_lds_dwordx4 %1, off\n\ts_mov_b32 m0, %0" : "=&s"(keep) : "v"(gsrc), "s"(lds_dst) : "memory");
}
DI void dma4(unsigned& lo, unsigned& hi, unsigned& lds, unsigned o0, unsigned o1, unsigned o2, unsigned o3) {
  unsigned keep; u64 sb = ((u64)hi << 32) | lo;
  asm volatile("s_mov_b32 %[keep], m0\n\ts_mov_b32 m0, %[lds]\n\ts_nop 4\n\t"
               "global_load_lds_dwordx4 %[o0], %[sb]\n\ts_add_u32 m0, m0, 0x1000\n\ts_nop 0\n\t"
               "global_load_lds_dwordx4 %[o1], %[sb]\n\ts_add_u32 m0, m0, 0x1000\n\ts_nop 0\n\t"
               "global_load_lds_dwordx4 %[o2], %[sb]\n\ts_add_u32 m0, m0, 0x1000\n\ts_nop 0\n\t"
               "global_load_lds_dwordx4 %[o3], %[sb]\n\ts_mov_b32 m0, %[keep]"
               : [keep] "=&s"(keep) : [lds] "s"(lds), [sb] "s"(sb), [o0] "v"(o0), [o1] "v"(o1), [o2] "v"(o2), [o3] "v"(o3) : "memory", "scc");
  sb += 16384; lo = (unsigned)sb; hi = (unsigned)(sb >> 32); lds += 16384;
}
#define LD128(off) __builtin_amdgcn_raw_buffer_load_b128(rs, (off), 0, AUX_SC1)
#define LD64(off) __builtin_amdgcn_raw_buffer_load_b64(rs, (off), 0, AUX_SC1)
#define ST128(v, off) __builtin_amdgcn_raw_buffer_store_b128((v), rs, (off), 0, AUX_SC1)
#define ST64(v, off) __builtin_amdgcn_raw_buffer_store_b64((v), rs, (off), 0, AUX_SC1)
#define VMWAIT(N) do { __builtin_amdgcn_sched_barrier(0); asm volatile("s_waitcnt vmcnt(%0)" :: "n"(N) : "memory"); __builtin_amdgcn_sched_barrier(0); } while (0)

__global__ void __launch_bounds__(1024) fused_kernel(Args a) {
  u64 pc0; asm volatile("s_getpc_b64 %0" : "=s"(pc0));
  extern __shared__ __attribute__((aligned(16))) unsigned char smem[];
  LAS unsigned char* L = (LAS unsigned char*)smem;
  const int tid = threadIdx.x, lane = tid & 63, wave = __builtin_amdgcn_readfirstlane(tid >> 6);
  const int wg = blockIdx.x, xl = wg & 7, r = wg >> 3;
  const int n = lane & 15, g = lane >> 4;
  const int h = xl + 8 * (r >> 3), j = r & 7, cq = j >> 1, dh = j & 1;
  const int hgA = r >> 4, kb = xl * 16 + (r & 15), cb = kb >> 6;
  const __amdgpu_buffer_rsrc_t rs = __builtin_amdgcn_make_buffer_rsrc(a.ws, 0, WS_END, 0x00020000);
  const Diag dg = make_diag(n);
  const f32x4 z4 = {0.f, 0.f, 0.f, 0.f};
  if (wave >= 8) {
    if (wave == 14) {
      const unsigned char* cp = (const unsigned char*)(pc0 & ~127ull) + (r * 8 + (lane & 7)) * 128;
      if (lane < 8 && (r * 8 + lane) * 128 < CODEPF) { const unsigned v = *(const volatile GAS unsigned*)cp; asm volatile("" :: "v"(v)); }
    } else if (wave == 12 || wave == 13) { const int t2 = tid - 768; if (t2 < (L_S1 - L_S0) / 16) *(LAS v4u*)(L + L_S0 + t2 * 16) = v4u{SENT, SENT, SENT, SENT}; }
    bar();
  }
  if (__builtin_expect(wave < 8, 1)) {
    const int w = wave;
    if (w == 0) { const unsigned char* xs = (const unsigned char*)a.x;
      glds16(xs + lane * 16, (unsigned)(size_t)(L + L_X)); glds16(xs + 1024 + lane * 16, (unsigned)(size_t)(L + L_X) + 1024);
      glds16(xs + 2048 + (lane < 16 ? lane : 15) * 16, (unsigned)(size_t)(L + L_X) + 2048); }
    v4u wv[9];
    { const u16* p = a.wq + (size_t)(2 * g + (n >> 3)) * 4096 + h * 128 + dh * 64 + 8 * (n & 7);
#pragma unroll
      for (int k = 0; k < 9; ++k) { int I = wave + 8 * k + 18 * cq; I = I >= 72 ? I - 72 : I; wv[k] = ldw(p + (size_t)I * (8 * 4096)); } }
    __builtin_amdgcn_sched_barrier(0);
    bar();
    __builtin_amdgcn_s_setprio(3);
    {
      if (w == 0) { VMWAIT(9); cnt_add(L, F_X, lane); }
      cnt_wait<1>(L, F_X, 1u);
      unsigned xv[9];
#pragma unroll
      for (int k = 0; k < 9; ++k) { int I = w + 8 * k + 18 * cq; I = I >= 72 ? I - 72 : I; xv[k] = *(const LAS unsigned*)(L + L_X + (n >> 3) * 1152 + (8 * I + 2 * g) * 2); }
      f32x4 a0 = z4, a1 = z4;
#pragma unroll
      for (int k = 0; k < 9; ++k) { a0 = mfma(diag(dg, xv[k] & 0xffffu), wv[k], a0); a1 = mfma(diag(dg, xv[k] >> 16), wv[k], a1); }
      f32x4 v = n < 8 ? a0 : a1;
#pragma unroll
      for (int k = 0; k < 4; ++k) v[k] += dppf<ROR8>(v[k]);
      if (n < 8) *(LAS f32x4*)(L + L_QP + ((w * 2 + (g >> 1)) * 64 + 8 * n + 4 * (g & 1)) * 4) = v;
      flag_set(L, F_QP + w, lane); }
    const u64 ep = __hip_atomic_load((const GAS u64*)(a.ws + WS_EPOCH), __ATOMIC_RELAXED, __HIP_MEMORY_SCOPE_AGENT) + EPOCH_STEP;
    const unsigned elo = (unsigned)ep, ehi = (unsigned)(ep >> 32);
    __builtin_amdgcn_s_setprio(0);
    cnt_wait<2>(L, F_SENT, 2u);
    cnt_wait<2>(L, F_DMAI, 4u);
    __builtin_amdgcn_s_sleep(SLH1);
    __builtin_amdgcn_s_setprio(3);
    v4u kf[16];
    { const int k8 = lane & 7, hq = lane >> 3;
      const int qb = WS_QA + ((cb * 32 + 16 * hgA + hq) * 8 + w) * 384 + k8 * 16, mo = WS_QA + ((cb * 32 + 16 * hgA + n) * 8 + w) * 384 + 256;
      v4u v0, v1, v2, v3; unsigned spins = 0;
      for (;;) {
        asm volatile("" ::: "memory");
        v0 = LD128(qb); v1 = LD128(qb + 128); v2 = LD128(qb + 8 * 3072); v3 = LD128(qb + 8 * 3072 + 128);
        const v4u mt = LD128(mo);
        __builtin_amdgcn_sched_barrier(0);
        if (spins == 0) {
          cnt_add(L, F_GO, lane);
        }
        __builtin_amdgcn_sched_barrier(0);
        unsigned U = 0, V = 0; cs(v0, U, V); cs(v1, U, V); cs(v2, U, V); cs(v3, U, V);
        if (lane < 16) { U -= mt.x + mt.z - 2u * elo; V -= mt.y + mt.w - 2u * ehi; }
        if ((wsum(U) | wsum(V)) == 0u || ++spins > SPIN_CAP) break;
        __builtin_amdgcn_s_sleep(6);
      }
      auto add2 = [](v4u p, v4u q) { v4u o;
        o.x = pk2(bflo(p.x) + bflo(q.x), bfhi(p.x) + bfhi(q.x)); o.y = pk2(bflo(p.y) + bflo(q.y), bfhi(p.y) + bfhi(q.y));
        o.z = pk2(bflo(p.z) + bflo(q.z), bfhi(p.z) + bfhi(q.z)); o.w = pk2(bflo(p.w) + bflo(q.w), bfhi(p.w) + bfhi(q.w)); return o; };
      const int wb = L_QA + (4 * (w >> 1) + (k8 & 3)) * 1024 + (2 * (w & 1) + (k8 >> 2)) * 256, rot = hq + 4 * (k8 & 3) + 2 * (w & 1);
      *(LAS v4u*)(L + wb + (rot & 15) * 16) = add2(v0, v1);
      *(LAS v4u*)(L + wb + ((rot + 8) & 15) * 16) = add2(v2, v3);
      flag_set(L, F_QA + w, lane);
    }
    flag_wait<4, SPN>(L, F_KV);
    {
          const int f = ((n & 3) << 2) | (n >> 2);
          const int rowb = L_KV + (16 * w + n) * 1024;
#pragma unroll
          for (int s = 0; s < 16; ++s) kf[s] = *(const LAS v4u*)(L + rowb + (s >> 2) * 256 + (((4 * g + (s & 3)) ^ f) * 16)); }
    __builtin_amdgcn_sched_barrier(0);
    flag_wait<8, SPN>(L, F_QA);
    f32x4 sc = z4, sc2 = z4;
#pragma unroll
    for (int hs = 0; hs < 2; ++hs) {
      v4u qf[8];
#pragma unroll
      for (int s = 0; s < 8; ++s) qf[s] = *(const LAS v4u*)(L + L_QA + (8 * hs + s) * 1024 + g * 256 + ((n + 4 * (s & 3) + 2 * (g >> 1)) & 15) * 16);
      __builtin_amdgcn_sched_barrier(0);
#pragma unroll
      for (int s = 0; s < 8; s += 2) { sc = mfma(kf[8 * hs + s], qf[s], sc); sc2 = mfma(kf[8 * hs + s + 1], qf[s + 1], sc2); }
    }
    sc += sc2;
    { float m = fmaxf(fmaxf(sc.x, sc.y), fmaxf(sc.z, sc.w)); m = x32maxf(x16maxf(m));
      if (g == 0) *(volatile LAS float*)(L + L_SM + n * 32 + w * 4) = m;
      flag_set(L, F_SM + w, lane); }
    const int q4 = n >> 2, p4 = n & 3;
    const unsigned base = L_KV + (8 * g + q4) * 1024 + (w >> 1) * 256 + ((((8 * (w & 1)) + (p4 >> 1)) ^ ((q4 << 2) | (2 * (g & 1)))) * 16) + 8 * (p4 & 1);
      bf16x8 vf[16];
#pragma unroll
      for (int ks = 0; ks < 4; ++ks)
#pragma unroll
        for (int T = 0; T < 4; ++T) {
          const s16x4 lo = __builtin_amdgcn_ds_read_tr16_b64_v4i16((LAS s16x4*)(L + ((base ^ (unsigned)(T << 5)) + ks * 32768)));
          const s16x4 hi = __builtin_amdgcn_ds_read_tr16_b64_v4i16((LAS s16x4*)(L + ((base ^ (unsigned)((T << 5) | (1 << 4) | (1 << 12))) + ks * 32768)));
          vf[ks * 4 + T] = __builtin_shufflevector(lo, hi, 0, 1, 2, 3, 4, 5, 6, 7); }
      __builtin_amdgcn_sched_barrier(0);
    float mcu;
    { v4u m0, m1;
      flag_wait<8, SPN>(L, F_SM); lds_rd2((unsigned)(size_t)L + L_SM + n * 32, m0, m1);
      const f32x4 f0 = __builtin_bit_cast(f32x4, m0), f1 = __builtin_bit_cast(f32x4, m1);
      mcu = fmaxf(fmaxf(fmaxf(f0.x, f0.y), fmaxf(f0.z, f0.w)), fmaxf(fmaxf(f1.x, f1.y), fmaxf(f1.z, f1.w)));
      const unsigned p01 = pk2(__builtin_amdgcn_exp2f(sc.x - mcu), __builtin_amdgcn_exp2f(sc.y - mcu));
      const unsigned p23 = pk2(__builtin_amdgcn_exp2f(sc.z - mcu), __builtin_amdgcn_exp2f(sc.w - mcu));
      *(volatile LAS v2u*)(L + L_P + (w >> 1) * 1024 + (n + 16 * (2 * (w & 1) + (g >> 1))) * 16 + 8 * (g & 1)) = v2u{p01, p23};
      float l = (bflo(p01) + bfhi(p01)) + (bflo(p23) + bfhi(p23)); l = x32addf(x16addf(l));
      if (g == 0) *(volatile LAS float*)(L + L_SS + n * 32 + w * 4) = l;
      flag_set(L, F_SS + w, lane); }
    {
      v4u l0, l1;
      flag_wait<8, SPN>(L, F_SS);
      l0 = *(const LAS v4u*)(L + L_SS + n * 32); l1 = *(const LAS v4u*)(L + L_SS + n * 32 + 16);
      f32x4 o[4] = {z4, z4, z4, z4};
      v4u pf[4];
#pragma unroll
      for (int ks = 0; ks < 4; ++ks) pf[ks] = *(const LAS v4u*)(L + L_P + ks * 1024 + lane * 16);
#pragma unroll
      for (int ks = 0; ks < 4; ++ks)
#pragma unroll
        for (int T = 0; T < 4; ++T) o[T] = __builtin_amdgcn_mfma_f32_16x16x32_bf16(vf[ks * 4 + T], __builtin_bit_cast(bf16x8, pf[ks]), o[T], 0, 0, 0);
      const f32x4 f0 = __builtin_bit_cast(f32x4, l0), f1 = __builtin_bit_cast(f32x4, l1);
      const float lcu = ((f0.x + f0.y) + (f0.z + f0.w)) + ((f1.x + f1.y) + (f1.z + f1.w));
      const int row = ((16 * hgA + n) * 4 + (w >> 1)) * 128 + kb;
      unsigned U = 0, V = 0;
#pragma unroll
      for (int T = 0; T < 4; T += 2) {
        unsigned a0 = pk2(o[T].x, o[T].y), a1 = pk2(o[T].z, o[T].w), b0 = pk2(o[T + 1].x, o[T + 1].y), b1 = pk2(o[T + 1].z, o[T + 1].w);
        auto s0 = __builtin_amdgcn_permlane16_swap(a0, b0, false, false); auto s1 = __builtin_amdgcn_permlane16_swap(a1, b1, false, false);
        const v4u ov = {s0[0], s1[0], s0[1], s1[1]};
        ST128(ov, WS_PO + row * 256 + (8 * (w & 1) + 2 * (T + (g & 1)) + (g >> 1)) * 16);
        cs(ov, U, V);
      }
      U = x32addu(x16addu(U)); V = x32addu(x16addu(V));
      const unsigned mb = __float_as_uint(mcu), lb = __float_as_uint(lcu);
      if (g == 0) ST128((v4u{mb, lb, d2(mb, 0x2B5D71C3u, d2(lb, 0x8F63E917u, U + elo)), d2(mb, 0xD1A34E8Bu, d2(lb, 0x47F92C6Du, V + ehi))}), WS_PM + (row * 2 + (w & 1)) * 16); }
    cnt_add(L, F_PUB, lane);
    v4u uv[2];
#pragma unroll
    for (int t = 0; t < 2; ++t) uv[t] = ldw(a.wuv + ((size_t)(128 * cq + 16 * w + 8 * t + 2 * g + (n >> 3)) * 32 + h) * 128 + 64 * dh + 8 * (n & 7));
    __builtin_amdgcn_sched_barrier(0);
    { const int row0 = (h * 4 + cq) * 128 + 8 * w + g;
      v4u p[4], mt[4]; unsigned spins = 0, need = 15u;
      __builtin_amdgcn_s_sleep(SLH2);
      for (;;) {
        asm volatile("" ::: "memory");
#pragma unroll
        for (int t = 0; t < 4; ++t) if (need & (1u << t)) { const int row = row0 + 4 * (t & 1) + 64 * (t >> 1); p[t] = LD128(WS_PO + row * 256 + n * 16); mt[t] = LD128(WS_PM + (row * 2 + (n >> 3)) * 16); }
        __builtin_amdgcn_sched_barrier(0);
        if (spins == 0) cnt_add(L, F_H2, lane);
        __builtin_amdgcn_sched_barrier(0);
#pragma unroll
        for (int t = 0; t < 4; ++t) if (need & (1u << t)) {
          unsigned U = 0, V = 0;
          if ((n & 7) == 0) { U = d2(mt[t].x, 0x2B5D71C3u, d2(mt[t].y, 0x8F63E917u, elo - mt[t].z)); V = d2(mt[t].x, 0xD1A34E8Bu, d2(mt[t].y, 0x47F92C6Du, ehi - mt[t].w)); }
          cs(p[t], U, V);
          U += dppu<XOR1>(U); V += dppu<XOR1>(V); U += dppu<XOR2>(U); V += dppu<XOR2>(V); U += dppu<HMIR>(U); V += dppu<HMIR>(V);
          if (__all((U | V) == 0u)) need &= ~(1u << t);
        }
        if (need == 0u || ++spins > SPIN_CAP) break;
        __builtin_amdgcn_s_sleep(SLT);
      }
      f32x4 acc = z4; float Mb[2], Lb[2];
#pragma unroll
      for (int b = 0; b < 2; ++b) {
        const float m0 = __uint_as_float(mt[2 * b].x), m1 = __uint_as_float(mt[2 * b + 1].x);
        Mb[b] = x32maxf(x16maxf(fmaxf(m0, m1)));
        const unsigned wp = pk2(__builtin_amdgcn_exp2f(m0 - Mb[b]), __builtin_amdgcn_exp2f(m1 - Mb[b]));
        const bool mine = (n >> 3) == b;
        acc = mfma(diag(dg, mine ? (wp & 0xffffu) : 0u), p[2 * b], acc); acc = mfma(diag(dg, mine ? (wp >> 16) : 0u), p[2 * b + 1], acc);
        Lb[b] = x32addf(x16addf(bflo(wp) * __uint_as_float(mt[2 * b].y) + bfhi(wp) * __uint_as_float(mt[2 * b + 1].y)));
      }
      *(volatile LAS f32x4*)(L + L_CP + ((w * 2 + (g >> 1)) * 128 + 8 * n + 4 * (g & 1)) * 4) = acc;
      if (lane < 2) *(volatile LAS v2u*)(L + L_CS + (lane * 8 + w) * 8) = v2u{__float_as_uint(lane ? Mb[1] : Mb[0]), __float_as_uint(lane ? Lb[1] : Lb[0])};
      flag_set(L, F_CS + w, lane); }
    { const int b = n >> 3; float fk[8];
      { v4u s[4]; unsigned gv;
        do { gv = *(volatile LAS unsigned*)(L + L_G + b * 4); } while (!__all(gv != SENT));
        flag_wait<8, SPN>(L, F_CS);
#pragma unroll
        for (int k = 0; k < 4; ++k) s[k] = *(const LAS v4u*)(L + L_CS + b * 64 + k * 16);
        f32x2 c[16];
        __builtin_amdgcn_sched_barrier(0);
        lds_rd16x2((unsigned)(size_t)L + L_CP + (b * 128 + 16 * w + 2 * g) * 4, c);
        float M = -INFINITY, Ls = 0.f;
#pragma unroll
        for (int k = 0; k < 4; ++k) M = fmaxf(M, fmaxf(__uint_as_float(s[k].x), __uint_as_float(s[k].z)));
#pragma unroll
        for (int k = 0; k < 4; ++k) { fk[2 * k] = __builtin_amdgcn_exp2f(__uint_as_float(s[k].x) - M); fk[2 * k + 1] = __builtin_amdgcn_exp2f(__uint_as_float(s[k].z) - M);
          Ls = fmaf(fk[2 * k], __uint_as_float(s[k].y), fmaf(fk[2 * k + 1], __uint_as_float(s[k].w), Ls)); }
        const float sg = __uint_as_float(gv) * __builtin_amdgcn_rcpf(Ls);
        f32x4 a0 = z4, a1 = z4;
#pragma unroll
        for (int t = 0; t < 2; ++t) { float s0 = 0.f, s1 = 0.f;
#pragma unroll
          for (int k = 0; k < 8; ++k) { s0 = fmaf(fk[k], c[t * 8 + k].x, s0); s1 = fmaf(fk[k], c[t * 8 + k].y, s1); }
          const unsigned sp = pk2(s0 * sg, s1 * sg);
          a0 = mfma(diag(dg, sp & 0xffffu), uv[t], a0); a1 = mfma(diag(dg, sp >> 16), uv[t], a1); }
        f32x4 v = n < 8 ? a0 : a1;
#pragma unroll
        for (int k = 0; k < 4; ++k) v[k] += dppf<ROR8>(v[k]);
        if (n < 8) *(volatile LAS f32x4*)(L + L_XP + ((w * 2 + (g >> 1)) * 64 + 8 * n + 4 * (g & 1)) * 4) = v; } }
    flag_set(L, F_XP + w, lane);
    if (w < 2) {
      flag_wait<8, SPN>(L, F_XP);
      float s = 0.f;
#pragma unroll
      for (int k = 0; k < 8; ++k) s += *(const LAS float*)(L + L_XP + ((k * 2 + w) * 64 + lane) * 4);
      *(volatile LAS unsigned*)(L + L_CTX + (w * 64 + (lane & 3) * 16 + (((lane >> 2) - 4 * cq) & 15)) * 4) = __float_as_uint(s);
      flag_set(L, F_CTX + w, lane);
    }
    if (w >= 2) {
      const int t6 = tid - 128, f = t6 >> 2, pt = t6 & 3, off = ((f / 48) * MD + 12 * (f % 48) + 4 * (pt < 3 ? pt : 0)) * 4;
      __builtin_amdgcn_s_setprio(0);
      flag_wait<8, 1>(L, F_OUT);
      __builtin_amdgcn_s_setprio(3);
      f32x4 s = *(const LAS f32x4*)(L + L_OT + off) + *(const LAS f32x4*)(L + L_OF + off);
      if (wg >= 216 && wg < 248) s += *(const LAS f32x4*)(L + L_OF2 + off);
      v4u o = __builtin_bit_cast(v4u, s);
      unsigned U = 0, V = 0; cs(o, U, V); if (pt == 3) { U = elo; V = ehi; }
      U += dppu<XOR1>(U); U += dppu<XOR2>(U); V += dppu<XOR1>(V); V += dppu<XOR2>(V);
      if (pt == 3) o = v4u{U, V, 0u, 0u};
      ST128(o, WS_FO + f * 16384 + wg * 64 + pt * 16);
    }
    if (wg < 96) {
      const int pt = lane & 3;
      v4u v0, v1; unsigned spins = 0;
      if (w < 2) flag_wait<8, 2>(L, F_OUT);
      __builtin_amdgcn_s_sleep(SLH3);
      for (;;) {
        asm volatile("" ::: "memory");
        v0 = LD128(WS_FO + wg * 16384 + tid * 16); v1 = LD128(WS_FO + wg * 16384 + 8192 + tid * 16);
        unsigned U = 0, V = 0; cs(v0, U, V); cs(v1, U, V);
        if (pt == 3) { U = 2u * elo - v0.x - v1.x; V = 2u * ehi - v0.y - v1.y; }
        if ((wsum(U) | wsum(V)) == 0u || ++spins > SPIN_CAP) break;
        __builtin_amdgcn_s_sleep(4);
      }
      f32x4 s = __builtin_bit_cast(f32x4, v0) + __builtin_bit_cast(f32x4, v1);
#pragma unroll
      for (int k = 0; k < 4; ++k) { float t = s[k]; t += dppf<ROR4>(t); t += dppf<ROR8>(t); t = x32addf(x16addf(t)); s[k] = t; }
      if (lane < 3) *(volatile LAS f32x4*)(L + L_FIN + w * 64 + lane * 16) = s;
      flag_set(L, F_FIN + w, lane);
      if (w == 0) {
        const int b = wg / 48, col = 12 * (wg % 48) + (lane < 12 ? lane : 0);
        const float bb = (1.f - *(const LAS float*)(L + L_G + b * 4)) * bf1(a.b2[col]);
        flag_wait<8, SPN>(L, F_FIN);
        if (lane < 12) {
          float t = bb;
#pragma unroll
          for (int k = 0; k < 8; ++k) t += *(const LAS float*)(L + L_FIN + k * 64 + lane * 4);
          a.out[b * MD + col] = (u16)bfr(t);
          if (wg == 0 && lane == 0) __hip_atomic_store((GAS u64*)(a.ws + WS_EPOCH), ep, __ATOMIC_RELAXED, __HIP_MEMORY_SCOPE_AGENT);
        }
      }
    }
  } else if (wave < 10) {
    const int kw = wave - 8;
    const u64 ep = __hip_atomic_load((const GAS u64*)(a.ws + WS_EPOCH), __ATOMIC_RELAXED, __HIP_MEMORY_SCOPE_AGENT) + EPOCH_STEP;
    const unsigned elo = (unsigned)ep, ehi = (unsigned)(ep >> 32);
    { v4u uk[8];
#pragma unroll
      for (int rg = 0; rg < 8; ++rg) uk[rg] = ldw(a.wuk + ((size_t)(128 * cq + 64 * kw + 8 * (n & 7) + rg) * 32 + h) * 128 + 64 * dh + 32 * (n >> 3) + 8 * g);
      __builtin_amdgcn_sched_barrier(0);
      cnt_add(L, F_ISS, lane);
      __builtin_amdgcn_s_setprio(3);
      flag_wait<8, SPN>(L, F_QP);
      { f32x2 s = {0.f, 0.f};
#pragma unroll
        for (int k = 0; k < 8; ++k) s += *(const LAS f32x2*)(L + L_QP + ((k * 2 + (lane >> 5)) * 64 + 2 * (lane & 31)) * 4);
        *(volatile LAS unsigned*)(L + L_QB + kw * 256 + lane * 4) = pk2(s.x, s.y); }
      v4u qa0 = lds_v4(L, L_QB + kw * 256 + (n & 1) * 128 + g * 16), qa1 = lds_v4(L, L_QB + kw * 256 + (n & 1) * 128 + 64 + g * 16);
      if (n >= 2) { qa0 = v4u{0u, 0u, 0u, 0u}; qa1 = qa0; }
      constexpr float SC = 0.08838834764831845f * 1.4426950408889634f;
      float sel[8];
#pragma unroll
      for (int rg = 0; rg < 8; ++rg) {
        const f32x4 a1 = mfma(qa0, uk[rg], z4), a2 = mfma(qa1, uk[rg], z4);
        float v0 = n < 8 ? a1.x : a2.x, v1 = n < 8 ? a1.y : a2.y;
        v0 += dppf<ROR8>(v0); v1 += dppf<ROR8>(v1);
        sel[rg] = (n < 8 ? v0 : v1) * SC;
      }
      const v4u o = {pk2(sel[0], sel[1]), pk2(sel[2], sel[3]), pk2(sel[4], sel[5]), pk2(sel[6], sel[7])};
      const int qb = WS_QA + (((n >> 3) * 32 + h) * 8 + 2 * cq + kw) * 384;
      if (lane < 16) ST128(o, qb + dh * 128 + (n & 7) * 16);
      unsigned U = 0, V = 0; cs(o, U, V);
      U += dppu<XOR1>(U); U += dppu<XOR2>(U); U += dppu<HMIR>(U); V += dppu<XOR1>(V); V += dppu<XOR2>(V); V += dppu<HMIR>(V);
      if (lane < 16 && (n & 7) == 0) ST64((v2u{U + elo, V + ehi}), qb + 256 + dh * 8);
      cnt_add(L, F_SENT, lane); }
    __builtin_amdgcn_s_setprio(0);
    cnt_wait<4>(L, F_PUB, 8u);
    f32x4 acc[3] = {z4, z4, z4};
    constexpr int pass = 0;
    {
      const int it = wg;
      const int hb = L_HID + pass * 128;
      v4u w2v[12];
      const u16* wp = a.w2 + (size_t)(16 * it + g) * MD + 384 * kw;
#pragma unroll
      for (int t = 0; t < 4; ++t) { w2v[t] = ldw(wp + (size_t)4 * t * MD + 8 * n); w2v[4 + t] = ldw(wp + (size_t)4 * t * MD + 128 + 8 * (kw ? (n & 7) : n)); }
      if (kw == 0) {
#pragma unroll
        for (int t = 0; t < 4; ++t) w2v[8 + t] = ldw(wp + (size_t)4 * t * MD + 256 + 8 * n);
        const v2u b1p = *(const v2u*)(a.b1 + 16 * it + (n & 1) * 8 + 4 * (g & 1));
        v4u v[6]; unsigned spins = 0;
        for (;;) {
          asm volatile("" ::: "memory");
#pragma unroll
          for (int k = 0; k < 6; ++k) v[k] = LD128(WS_HX + ((it >> 3) * 6 + k) * 1152 + lane * 16);
          const v2u mt = LD64(WS_HX + ((it >> 3) * 6 + (lane < 6 ? lane : 0)) * 1152 + 1024);
          unsigned U = 0, V = 0;
#pragma unroll
          for (int k = 0; k < 6; ++k) cs(v[k], U, V);
          if (lane < 6) { U -= mt.x - elo; V -= mt.y - ehi; }
          if ((wsum(U) | wsum(V)) == 0u || ++spins > SPIN_CAP) break;
          __builtin_amdgcn_s_sleep(10);
        }
        unsigned gv; do { gv = *(volatile LAS unsigned*)(L + L_G + (g >> 1) * 4); } while (!__all(gv != SENT));
        f32x4 s = z4;
#pragma unroll
        for (int k = 0; k < 6; ++k) s += __builtin_bit_cast(f32x4, v[k]);
        if ((n >> 1) == (it & 7)) {
          const float og = 1.f - __uint_as_float(gv);
          f32x4 hv; hv.x = fmaxf(s.x + bflo(b1p.x), 0.f); hv.y = fmaxf(s.y + bfhi(b1p.x), 0.f); hv.z = fmaxf(s.z + bflo(b1p.y), 0.f); hv.w = fmaxf(s.w + bfhi(b1p.y), 0.f);
          *(volatile LAS f32x4*)(L + hb + ((g >> 1) * 16 + 8 * (n & 1) + 4 * (g & 1)) * 4) = hv * hv * og;
        }
        cnt_add(L, F_HID, lane);
      } else cnt_wait<4>(L, F_HID, (unsigned)pass + 1u);
#pragma unroll
      for (int t = 0; t < 4; ++t) { const v4u d = diag(dg, bfr(*(volatile LAS float*)(L + hb + ((n >> 3) * 16 + 4 * t + g) * 4)));
        acc[0] = mfma(d, w2v[t], acc[0]); acc[1] = mfma(d, w2v[4 + t], acc[1]); if (kw == 0) acc[2] = mfma(d, w2v[8 + t], acc[2]); }
    }
    { const int ob = L_OF + ((g >> 1) * MD + 384 * kw + 8 * n + 4 * (g & 1)) * 4;
      *(volatile LAS f32x4*)(L + ob) = acc[0];
      if (kw == 0 || n < 8) *(volatile LAS f32x4*)(L + ob + 512) = acc[1];
      if (kw == 0) *(volatile LAS f32x4*)(L + ob + 1024) = acc[2]; }
    flag_set(L, F_OUT + (5 + kw), lane);
  } else if (wave < 15) {
    const int T = wave - 10;
    const u16* p = a.wo + (size_t)(h * 128 + 64 * dh) * MD;
    if (T < 4) {
      cnt_wait<1>(L, F_ISS, 2u);
      {
        const unsigned o0 = ((lane ^ (T << 2)) * 16), o1 = 4096 + ((lane ^ ((T << 2) | 1)) * 16), o2 = 8192 + ((lane ^ ((T << 2) | 2)) * 16), o3 = 12288 + ((lane ^ ((T << 2) | 3)) * 16);
#pragma unroll
        for (int hf = 0; hf < 2; ++hf) {
          const int half = hf ^ hgA;
          const u64 sb = (u64)(size_t)a.ckv + (u64)(kb * 128 + 64 * half + T) * 1024;
          unsigned lo = __builtin_amdgcn_readfirstlane((unsigned)sb), hi = __builtin_amdgcn_readfirstlane((unsigned)(sb >> 32));
          unsigned ld = __builtin_amdgcn_readfirstlane((unsigned)(size_t)(L + L_KV) + (64 * half + T) * 1024);
#pragma unroll
          for (int q = 0; q < 4; ++q) { dma4(lo, hi, ld, o0, o1, o2, o3);
            if (hf == 0 && 4 * q < NTH) asm volatile("s_waitcnt vmcnt(%0)" :: "n"(WKV - 1) : "memory"); }
        } }
      cnt_add(L, F_DMAI, lane);
      VMWAIT(0);
      flag_set(L, F_KV + T, lane);
      cnt_wait<2>(L, F_GO, 8u);
      v4u wo[16];
#pragma unroll
      for (int t = 0; t < 16; ++t) {
        if (t == ST1) { __builtin_amdgcn_sched_barrier(0); cnt_wait<3>(L, F_PUB, 8u); }
        if (t == ST2) { __builtin_amdgcn_sched_barrier(0); cnt_wait<3>(L, F_H2, 8u); }
        wo[t] = ldw(p + (size_t)(4 * ((t + 4 * cq) & 15) + g) * MD + 128 * T + 8 * n); }
      __builtin_amdgcn_sched_barrier(0);
      flag_wait<1, 4>(L, F_CS);
      __builtin_amdgcn_s_setprio(3);
      f32x4 acc = z4; v4u c[4];
      flag_wait<2, SPN>(L, F_CTX);
      lds_rd4((unsigned)(size_t)L + L_CTX + ((n >> 3) * 64 + g * 16) * 4, c[0], c[1], c[2], c[3]);
#pragma unroll
      for (int t4 = 0; t4 < 4; ++t4) { const unsigned c01 = pk2(__uint_as_float(c[t4].x), __uint_as_float(c[t4].y)), c23 = pk2(__uint_as_float(c[t4].z), __uint_as_float(c[t4].w));
        acc = mfma(diag(dg, c01 & 0xffffu), wo[4 * t4], acc); acc = mfma(diag(dg, c01 >> 16), wo[4 * t4 + 1], acc);
        acc = mfma(diag(dg, c23 & 0xffffu), wo[4 * t4 + 2], acc); acc = mfma(diag(dg, c23 >> 16), wo[4 * t4 + 3], acc); }
      *(volatile LAS f32x4*)(L + L_OT + ((g >> 1) * MD + 128 * T + 8 * n + 4 * (g & 1)) * 4) = acc;
    } else {
      cnt_wait<4>(L, F_PUB, 8u);
      v4u wo[8];
#pragma unroll
      for (int t = 0; t < 8; ++t) wo[t] = ldw(p + (size_t)(8 * ((t + 2 * cq) & 7) + 2 * g + (n >> 3)) * MD + 512 + 8 * (n & 7));
      __builtin_amdgcn_sched_barrier(0);
      flag_wait<1, 4>(L, F_CS);
      __builtin_amdgcn_s_setprio(3);
      v4u c[8];
      flag_wait<2, SPN>(L, F_CTX);
      lds_rd8((unsigned)(size_t)L + L_CTX + ((n >> 3) * 64 + (2 * (g & 1)) * 16) * 4, c);
      f32x4 a0 = z4, a1 = z4;
#pragma unroll
      for (int t = 0; t < 8; ++t) { const int e = 2 * (t & 1);
        const unsigned u0 = (g >> 1) ? c[t >> 1][e + 1] : c[t >> 1][e], u1 = (g >> 1) ? c[4 + (t >> 1)][e + 1] : c[4 + (t >> 1)][e];
        const unsigned cp = pk2(__uint_as_float(u0), __uint_as_float(u1));
        a0 = mfma(diag(dg, cp & 0xffffu), wo[t], a0); a1 = mfma(diag(dg, cp >> 16), wo[t], a1); }
      f32x4 v = n < 8 ? a0 : a1;
#pragma unroll
      for (int k = 0; k < 4; ++k) v[k] += dppf<ROR8>(v[k]);
      if (n < 8) *(volatile LAS f32x4*)(L + L_OT + ((g >> 1) * MD + 512 + 8 * n + 4 * (g & 1)) * 4) = v;
    }
    flag_set(L, F_OUT + T, lane);
  } else {
    cnt_wait<2>(L, F_X, 1u);
    const u64 ep = __hip_atomic_load((const GAS u64*)(a.ws + WS_EPOCH), __ATOMIC_RELAXED, __HIP_MEMORY_SCOPE_AGENT) + EPOCH_STEP;
    const unsigned elo = (unsigned)ep, ehi = (unsigned)(ep >> 32);
    { float s0 = 0.f, s1 = 0.f;
      if (lane < 36) {
        const v4u gw = *(const v4u*)(a.gw + 8 * lane), gw2 = *(const v4u*)(a.gw + 288 + 8 * lane);
        const v4u xa = *(const LAS v4u*)(L + L_X + 16 * lane), xa2 = *(const LAS v4u*)(L + L_X + 576 + 16 * lane), xb = *(const LAS v4u*)(L + L_X + 1152 + 16 * lane), xb2 = *(const LAS v4u*)(L + L_X + 1728 + 16 * lane);
#pragma unroll
        for (int k = 0; k < 4; ++k) {
          s0 += bflo(gw[k]) * bflo(xa[k]) + bfhi(gw[k]) * bfhi(xa[k]) + bflo(gw2[k]) * bflo(xa2[k]) + bfhi(gw2[k]) * bfhi(xa2[k]);
          s1 += bflo(gw[k]) * bflo(xb[k]) + bfhi(gw[k]) * bfhi(xb[k]) + bflo(gw2[k]) * bflo(xb2[k]) + bfhi(gw2[k]) * bfhi(xb2[k]); }
      }
      s0 = wsumf(s0); s1 = wsumf(s1);
      if (lane < 2) *(volatile LAS unsigned*)(L + L_G + lane * 4) = min(__float_as_uint(1.f / (1.f + __expf(-(lane ? s1 : s0)))), SENT - 1u); }
    if (wg < 216) {
      const int T = wg / 6, rg = wg % 6;
      cnt_wait<2>(L, F_GO, 8u);
      const u16* p = a.w1 + (size_t)(96 * rg + 24 * g) * NF + 128 * T + 8 * n;
      v4u wv[24];
#pragma unroll
      for (int t = 0; t < 24; ++t) wv[t] = ldw(p + (size_t)t * NF);
      __builtin_amdgcn_sched_barrier(0);
      v4u xs[3];
#pragma unroll
      for (int k = 0; k < 3; ++k) xs[k] = *(const LAS v4u*)(L + L_X + (n >> 3) * 1152 + (96 * rg + 24 * g) * 2 + 16 * k);
      f32x4 acc = z4;
#pragma unroll
      for (int t = 0; t < 24; ++t) { const unsigned xd = xs[t >> 3][(t >> 1) & 3]; acc = mfma(diag(dg, (t & 1) ? (xd >> 16) : (xd & 0xffffu)), wv[t], acc); }
      const v4u o = __builtin_bit_cast(v4u, acc);
      ST128(o, WS_HX + wg * 1152 + lane * 16);
      unsigned U = 0, V = 0; cs(o, U, V); U = wsum(U); V = wsum(V);
      if (lane == 0) ST64((v2u{U + elo, V + ehi}), WS_HX + wg * 1152 + 1024);
    } else if (wg < 248) {
      const int it = 40 + wg;
      cnt_wait<4>(L, F_GO, 8u);
      v4u w2v[20];
      const u16* wp = a.w2 + (size_t)(16 * it + g) * MD;
#pragma unroll
      for (int t = 0; t < 4; ++t)
#pragma unroll
        for (int T = 0; T < 4; ++T) w2v[T * 4 + t] = ldw(wp + (size_t)4 * t * MD + 128 * T + 8 * n);
      const v2u b1p = *(const v2u*)(a.b1 + 16 * it + (n & 1) * 8 + 4 * (g & 1));
      __builtin_amdgcn_sched_barrier(0);
      cnt_wait<4>(L, F_PUB, 8u);
      f32x4 sacc; unsigned spins = 0;
      for (;;) {
        asm volatile("" ::: "memory");
        unsigned U = 0, V = 0; sacc = z4;
#pragma unroll
        for (int k = 0; k < 6; k += 2) { const v4u v0 = LD128(WS_HX + ((it >> 3) * 6 + k) * 1152 + lane * 16), v1 = LD128(WS_HX + ((it >> 3) * 6 + k + 1) * 1152 + lane * 16);
          cs(v0, U, V); cs(v1, U, V); sacc += __builtin_bit_cast(f32x4, v0); sacc += __builtin_bit_cast(f32x4, v1); __builtin_amdgcn_sched_barrier(0); }
        const v2u mt = LD64(WS_HX + ((it >> 3) * 6 + (lane < 6 ? lane : 0)) * 1152 + 1024);
        if (lane < 6) { U -= mt.x - elo; V -= mt.y - ehi; }
        if ((wsum(U) | wsum(V)) == 0u || ++spins > SPIN_CAP) break;
        __builtin_amdgcn_s_sleep(20);
      }
#pragma unroll
      for (int t = 0; t < 4; ++t) w2v[16 + t] = ldw(wp + (size_t)4 * t * MD + 512 + 8 * (n & 7));
      if ((n >> 1) == (it & 7)) {
        const float og = 1.f - *(volatile LAS float*)(L + L_G + (g >> 1) * 4);
        f32x4 hv; hv.x = fmaxf(sacc.x + bflo(b1p.x), 0.f); hv.y = fmaxf(sacc.y + bfhi(b1p.x), 0.f); hv.z = fmaxf(sacc.z + bflo(b1p.y), 0.f); hv.w = fmaxf(sacc.w + bfhi(b1p.y), 0.f);
        *(volatile LAS f32x4*)(L + L_HID + 128 + ((g >> 1) * 16 + 8 * (n & 1) + 4 * (g & 1)) * 4) = hv * hv * og;
      }
      unsigned hb4[4];
#pragma unroll
      for (int t = 0; t < 4; ++t) hb4[t] = bfr(*(volatile LAS float*)(L + L_HID + 128 + ((n >> 3) * 16 + 4 * t + g) * 4));
#pragma unroll
      for (int T = 0; T < 5; ++T) { f32x4 acc = z4;
#pragma unroll
        for (int t = 0; t < 4; ++t) acc = mfma(diag(dg, hb4[t]), w2v[T * 4 + t], acc);
        if (T < 4 || n < 8) *(volatile LAS f32x4*)(L + L_OF2 + ((g >> 1) * MD + 128 * T + 8 * n + 4 * (g & 1)) * 4) = acc;
        __builtin_amdgcn_sched_barrier(0); }
    }
    flag_set(L, F_OUT + 7, lane);
  }
}
}

extern "C" void kernel_launch(void* const* d_in, const int* in_sizes, int n_in, void* d_out, int out_size, void* d_ws, size_t ws_size, hipStream_t stream) {
  Args a;
  a.x = (const u16*)d_in[0]; a.ckv = (const u16*)d_in[1]; a.wq = (const u16*)d_in[2]; a.wuk = (const u16*)d_in[3]; a.wuv = (const u16*)d_in[4]; a.wo = (const u16*)d_in[5];
  a.w1 = (const u16*)d_in[7]; a.b1 = (const u16*)d_in[8]; a.w2 = (const u16*)d_in[9]; a.b2 = (const u16*)d_in[10]; a.gw = (const u16*)d_in[11];
  a.out = (u16*)d_out; a.ws = (unsigned char*)d_ws;
  (void)hipFuncSetAttribute((const void*)fused_kernel, hipFuncAttributeMaxDynamicSharedMemorySize, L_END);
  hipLaunchKernelGGL(fused_kernel, dim3(256), dim3(1024), L_END, stream, a);
}
```
